```python
import torch

def _softmax(x, axis=-1):
    return torch.softmax(x, dim=axis)
def _gelu(x):
    return torch.nn.functional.gelu(x, approximate="tanh")
def _silu(x):
    return torch.nn.functional.silu(x)
def _sigmoid(x):
    return torch.sigmoid(x)
def _layernorm(x, axis=-1, eps=1e-5):
    m = x.mean(dim=axis, keepdim=True)
    v = x.var(dim=axis, keepdim=True, unbiased=False)
    return (x - m) / torch.sqrt(v + eps)
def _scatter_add(tgt, idx, src, axis=0):
    return tgt.clone().index_add_(axis, idx, src)
def _segment_sum(src, seg, n):
    out = torch.zeros((n,) + src.shape[1:], dtype=src.dtype)
    return out.index_add_(0, seg, src)
def _pad(x, pw):
    flat = tuple(v for pair in reversed(pw) for v in pair)
    return torch.nn.functional.pad(x, flat)

kernel_name = 'concat_heads__ssm_conv_s4__spectral_gfnet__prompt'

def setup_inputs():

    _w = (lambda a: (lambda t: t.to(torch.bfloat16) if t.is_floating_point() else t)(torch.as_tensor(a)))
    def _A_setup():
        B, L, D, N = 1, 2048, 1024, 128
        A_re = -(lambda _x: torch.exp(torch.as_tensor(_x)))((lambda *s: torch.randn(*s, dtype=torch.bfloat16) * 3.2)(D, N))
        A_im = torch.pi * (lambda *s: torch.randn(*s, dtype=torch.bfloat16) * 0.05)(D, N)
        B_re = (lambda *s: torch.randn(*s, dtype=torch.bfloat16) * 0.1)(D, N)
        B_im = (lambda *s: torch.randn(*s, dtype=torch.bfloat16) * 0.1)(D, N)
        C_re = (lambda *s: torch.randn(*s, dtype=torch.bfloat16) * 0.1)(D, N)
        C_im = (lambda *s: torch.randn(*s, dtype=torch.bfloat16) * 0.1)(D, N)
        log_dt = (lambda *s: torch.randn(*s, dtype=torch.bfloat16) * 0.05)(D) * 0.5 - 4.0
        Dskip = (lambda *s: torch.randn(*s, dtype=torch.bfloat16) * 0.05)(D)
        return {
            "u": _w((lambda *s: torch.randn(*s, dtype=torch.bfloat16) * 0.05)(B, L, D)),
            "A_re": _w(A_re),
            "A_im": _w(A_im),
            "B_re": _w(B_re),
            "B_im": _w(B_im),
            "C_re": _w(C_re),
            "C_im": _w(C_im),
            "log_dt": _w(log_dt),
            "Dskip": _w(Dskip),
        }
    _A_in = _A_setup()

    def _B_setup():
        B, S, D = 1, 2048, 1024
        Df = D // 2 + 1
        return {
            "x": _w((lambda *s: torch.randn(*s, dtype=torch.bfloat16) * 0.05)(B, S, D)),
            "w_r": _w((lambda *s: torch.randn(*s, dtype=torch.bfloat16) * 0.8)(S, Df) * 0.02),
            "w_i": _w((lambda *s: torch.randn(*s, dtype=torch.bfloat16) * 0.8)(S, Df) * 0.02),
        }
    _B_in = _B_setup()

    return {
        "A_u": _A_in["u"],
        "A_A_re": _A_in["A_re"],
        "A_A_im": _A_in["A_im"],
        "A_B_re": _A_in["B_re"],
        "A_B_im": _A_in["B_im"],
        "A_C_re": _A_in["C_re"],
        "A_C_im": _A_in["C_im"],
        "A_log_dt": _A_in["log_dt"],
        "A_Dskip": _A_in["Dskip"],
        "B_x": _B_in["x"],
        "B_w_r": _B_in["w_r"],
        "B_w_i": _B_in["w_i"],
    }

def reference(A_u, A_A_re, A_A_im, A_B_re, A_B_im, A_C_re, A_C_im, A_log_dt, A_Dskip, B_x, B_w_r, B_w_i):
    def _A_ref(u, A_re, A_im, B_re, B_im, C_re, C_im, log_dt, Dskip):
        Bsz, L, D = u.shape
        dt = torch.exp(log_dt)[:, None]
        A = A_re + 1j * A_im
        Bc = B_re + 1j * B_im
        Cc = C_re + 1j * C_im
        logdA = dt * A
        dB = dt * Bc
        l = (lambda x, dt: x.to(dt))(torch.arange(L), A_re.dtype)
        P = torch.exp(logdA[:, :, None] * l[None, None, :])
        K = torch.einsum("dn,dnl,dn->dl", Cc, P, dB).real
        L2 = 2 * L
        Kp = _pad(K, ((0, 0), (0, L)))
        up = (lambda x, *d: x.permute(*d))(u, 0, 2, 1)
        up = _pad(up, ((0, 0), (0, 0), (0, L)))
        Kf = (lambda x, axes=(-2,-1): torch.fft.rfft2(x.float(), dim=axes))(Kp[:, None, :], axes=(-2, -1))
        Uf = (lambda x, axes=(-2,-1): torch.fft.rfft2(x.float(), dim=axes))(up[:, :, None, :], axes=(-2, -1))
        Yf = Uf * Kf[None, :, :, :]
        y = (lambda x, s, axes=(-2,-1): torch.fft.irfft2(x, s=s, dim=axes).to(torch.bfloat16))(Yf, (1, L2), axes=(-2, -1))[:, :, 0, :L]
        y = (lambda x, *d: x.permute(*d))(y, 0, 2, 1) + u * Dskip[None, None, :]
        return y

    def _B_ref(x, w_r, w_i):
        B, S, D = x.shape
        X = (lambda x, axes=(-2,-1): torch.fft.rfft2(x.float(), dim=axes))(x, axes=(-2, -1))
        W = w_r + 1j * w_i
        X = X * W
        y = (lambda x, s, axes=(-2,-1): torch.fft.irfft2(x, s=s, dim=axes).to(torch.bfloat16))(X, (S, D), axes=(-2, -1))
        return y

    a = _A_ref(A_u, A_A_re, A_A_im, A_B_re, A_B_im, A_C_re, A_C_im, A_log_dt, A_Dskip)
    b = _B_ref(B_x, B_w_r, B_w_i)
    return torch.concatenate([a, b], axis=-1)
```

```cpp
#include <hip/hip_runtime.h>
#include <hip/hip_bf16.h>
#include <stdint.h>
#pragma clang diagnostic ignored "-Wint-to-pointer-cast"
#pragma clang diagnostic ignored "-Warray-bounds"

typedef __attribute__((ext_vector_type(2))) float cf;
typedef __attribute__((ext_vector_type(4))) float f32x4;
typedef __attribute__((ext_vector_type(2))) uint32_t u32x2;
typedef __attribute__((ext_vector_type(4))) uint32_t u32x4;
typedef __attribute__((ext_vector_type(2))) __bf16 bf16x2;
typedef __attribute__((ext_vector_type(8))) __bf16 bf16x8;

__device__ const __attribute__((aligned(256))) cf TW[384] = {
{0x1.000000p+0f,0.f},{0x1.fffd88p-1f,-0x1.921f10p-8f},{0x1.fff622p-1f,-0x1.921d20p-7f},{0x1.ffe9ccp-1f,-0x1.2d936cp-6f},
{0x1.ffd886p-1f,-0x1.921560p-6f},{0x1.ffc252p-1f,-0x1.f69374p-6f},{0x1.ffa72ep-1f,-0x1.2d8658p-5f},{0x1.ff871ep-1f,-0x1.5fc00ep-5f},
{0x1.ff621ep-1f,-0x1.91f660p-5f},{0x1.ff3830p-1f,-0x1.c428d2p-5f},{0x1.ff0956p-1f,-0x1.f656e8p-5f},{0x1.fed58ep-1f,-0x1.144014p-4f},
{0x1.fe9cdap-1f,-0x1.2d520ap-4f},{0x1.fe5f3ap-1f,-0x1.466118p-4f},{0x1.fe1cb0p-1f,-0x1.5f6d00p-4f},{0x1.fdd53ap-1f,-0x1.787586p-4f},
{0x1.fd88dap-1f,-0x1.917a6cp-4f},{0x1.fd3792p-1f,-0x1.aa7b72p-4f},{0x1.fce160p-1f,-0x1.c3785cp-4f},{0x1.fc8646p-1f,-0x1.dc70ecp-4f},
{0x1.fc2648p-1f,-0x1.f564e6p-4f},{0x1.fbc162p-1f,-0x1.072a04p-3f},{0x1.fb5798p-1f,-0x1.139f0cp-3f},{0x1.fae8e8p-1f,-0x1.20116ep-3f},
{0x1.fa7558p-1f,-0x1.2c8106p-3f},{0x1.f9fce6p-1f,-0x1.38edbcp-3f},{0x1.f97f92p-1f,-0x1.45576cp-3f},{0x1.f8fd60p-1f,-0x1.51bdf8p-3f},
{0x1.f87650p-1f,-0x1.5e2144p-3f},{0x1.f7ea62p-1f,-0x1.6a8130p-3f},{0x1.f7599ap-1f,-0x1.76dd9ep-3f},{0x1.f6c3f8p-1f,-0x1.83366ep-3f},
{0x1.f6297cp-1f,-0x1.8f8b84p-3f},{0x1.f58a2cp-1f,-0x1.9bdcc0p-3f},{0x1.f4e604p-1f,-0x1.a82a02p-3f},{0x1.f43d08p-1f,-0x1.b4732ep-3f},
{0x1.f38f3ap-1f,-0x1.c0b826p-3f},{0x1.f2dc9cp-1f,-0x1.ccf8ccp-3f},{0x1.f22530p-1f,-0x1.d934fep-3f},{0x1.f168f6p-1f,-0x1.e56ca2p-3f},
{0x1.f0a7f0p-1f,-0x1.f19f98p-3f},{0x1.efe220p-1f,-0x1.fdcdc2p-3f},{0x1.ef178ap-1f,-0x1.04fb80p-2f},{0x1.ee482ep-1f,-0x1.0b0d9cp-2f},
{0x1.ed740ep-1f,-0x1.111d26p-2f},{0x1.ec9b2ep-1f,-0x1.172a0ep-2f},{0x1.ebbd8cp-1f,-0x1.1d3444p-2f},{0x1.eadb2ep-1f,-0x1.233bbap-2f},
{0x1.e9f416p-1f,-0x1.294062p-2f},{0x1.e90844p-1f,-0x1.2f422ep-2f},{0x1.e817bap-1f,-0x1.35410cp-2f},{0x1.e7227ep-1f,-0x1.3b3cf0p-2f},
{0x1.e6288ep-1f,-0x1.4135cap-2f},{0x1.e529f0p-1f,-0x1.472b8ap-2f},{0x1.e426a4p-1f,-0x1.4d1e24p-2f},{0x1.e31eaep-1f,-0x1.530d88p-2f},
{0x1.e21210p-1f,-0x1.58f9a8p-2f},{0x1.e100ccp-1f,-0x1.5ee274p-2f},{0x1.dfeae6p-1f,-0x1.64c7dep-2f},{0x1.ded060p-1f,-0x1.6aa9d8p-2f},
{0x1.ddb13cp-1f,-0x1.708854p-2f},{0x1.dc8d7cp-1f,-0x1.766340p-2f},{0x1.db6526p-1f,-0x1.7c3a94p-2f},{0x1.da383ap-1f,-0x1.820e3cp-2f},
{0x1.d906bcp-1f,-0x1.87de2ap-2f},{0x1.d7d0b0p-1f,-0x1.8daa52p-2f},{0x1.d69618p-1f,-0x1.9372a6p-2f},{0x1.d556f6p-1f,-0x1.993716p-2f},
{0x1.d4134ep-1f,-0x1.9ef794p-2f},{0x1.d2cb22p-1f,-0x1.a4b412p-2f},{0x1.d17e78p-1f,-0x1.aa6c82p-2f},{0x1.d02d50p-1f,-0x1.b020d6p-2f},
{0x1.ced7b0p-1f,-0x1.b5d100p-2f},{0x1.cd7d98p-1f,-0x1.bb7cf2p-2f},{0x1.cc1f10p-1f,-0x1.c1249ep-2f},{0x1.cabc16p-1f,-0x1.c6c7f4p-2f},
{0x1.c954b2p-1f,-0x1.cc66eap-2f},{0x1.c7e8e6p-1f,-0x1.d2016ep-2f},{0x1.c678b4p-1f,-0x1.d79776p-2f},{0x1.c50420p-1f,-0x1.dd28f2p-2f},
{0x1.c38b30p-1f,-0x1.e2b5d4p-2f},{0x1.c20de4p-1f,-0x1.e83e0ep-2f},{0x1.c08c42p-1f,-0x1.edc196p-2f},{0x1.bf064ep-1f,-0x1.f3405ap-2f},
{0x1.bd7c0ap-1f,-0x1.f8ba4ep-2f},{0x1.bbed7cp-1f,-0x1.fe2f64p-2f},{0x1.ba5aa6p-1f,-0x1.01cfc8p-1f},{0x1.b8c38ep-1f,-0x1.048562p-1f},
{0x1.b72834p-1f,-0x1.07387ap-1f},{0x1.b588a0p-1f,-0x1.09e908p-1f},{0x1.b3e4d4p-1f,-0x1.0c9704p-1f},{0x1.b23cd4p-1f,-0x1.0f426cp-1f},
{0x1.b090a6p-1f,-0x1.11eb36p-1f},{0x1.aee04cp-1f,-0x1.14915ap-1f},{0x1.ad2bcap-1f,-0x1.1734d6p-1f},{0x1.ab7326p-1f,-0x1.19d5a0p-1f},
{0x1.a9b662p-1f,-0x1.1c73b4p-1f},{0x1.a7f586p-1f,-0x1.1f0f08p-1f},{0x1.a63092p-1f,-0x1.21a79ap-1f},{0x1.a4678cp-1f,-0x1.243d60p-1f},
{0x1.a29a7ap-1f,-0x1.26d054p-1f},{0x1.a0c95ep-1f,-0x1.296072p-1f},{0x1.9ef43ep-1f,-0x1.2bedb2p-1f},{0x1.9d1b20p-1f,-0x1.2e780ep-1f},
{0x1.9b3e04p-1f,-0x1.30ff80p-1f},{0x1.995cf2p-1f,-0x1.338400p-1f},{0x1.9777f0p-1f,-0x1.36058cp-1f},{0x1.958efep-1f,-0x1.388418p-1f},
{0x1.93a224p-1f,-0x1.3affa2p-1f},{0x1.91b166p-1f,-0x1.3d7824p-1f},{0x1.8fbccap-1f,-0x1.3fed96p-1f},{0x1.8dc454p-1f,-0x1.425ff2p-1f},
{0x1.8bc806p-1f,-0x1.44cf32p-1f},{0x1.89c7eap-1f,-0x1.473b52p-1f},{0x1.87c400p-1f,-0x1.49a44ap-1f},{0x1.85bc52p-1f,-0x1.4c0a14p-1f},
{0x1.83b0e0p-1f,-0x1.4e6cacp-1f},{0x1.81a1b4p-1f,-0x1.50cc0ap-1f},{0x1.7f8ecep-1f,-0x1.53282ap-1f},{0x1.7d7836p-1f,-0x1.558104p-1f},
{0x1.7b5df2p-1f,-0x1.57d694p-1f},{0x1.794006p-1f,-0x1.5a28d2p-1f},{0x1.771e76p-1f,-0x1.5c77bcp-1f},{0x1.74f948p-1f,-0x1.5ec34ap-1f},
{0x1.72d084p-1f,-0x1.610b76p-1f},{0x1.70a42cp-1f,-0x1.63503ap-1f},{0x1.6e7446p-1f,-0x1.659192p-1f},{0x1.6c40d8p-1f,-0x1.67cf78p-1f},
{0x1.000000p+0f,0.f},{0x1.ffff62p-1f,-0x1.921f8cp-9f},{0x1.fffd88p-1f,-0x1.921f10p-8f},{0x1.fffa72p-1f,-0x1.2d96b0p-7f},
{0x1.fff622p-1f,-0x1.921d20p-7f},{0x1.fff094p-1f,-0x1.f6a296p-7f},{0x1.ffe9ccp-1f,-0x1.2d936cp-6f},{0x1.ffe1c6p-1f,-0x1.5fd4d2p-6f},
{0x1.ffd886p-1f,-0x1.921560p-6f},{0x1.ffce0ap-1f,-0x1.c454f4p-6f},{0x1.ffc252p-1f,-0x1.f69374p-6f},{0x1.ffb55ep-1f,-0x1.14685ep-5f},
{0x1.ffa72ep-1f,-0x1.2d8658p-5f},{0x1.ff97c4p-1f,-0x1.46a396p-5f},{0x1.ff871ep-1f,-0x1.5fc00ep-5f},{0x1.ff753cp-1f,-0x1.78dbaap-5f},
{0x1.ff621ep-1f,-0x1.91f660p-5f},{0x1.ff4dc6p-1f,-0x1.ab101cp-5f},{0x1.ff3830p-1f,-0x1.c428d2p-5f},{0x1.ff2162p-1f,-0x1.dd4070p-5f},
{0x1.ff0956p-1f,-0x1.f656e8p-5f},{0x1.fef010p-1f,-0x1.07b614p-4f},{0x1.fed58ep-1f,-0x1.144014p-4f},{0x1.feb9d2p-1f,-0x1.20c968p-4f},
{0x1.fe9cdap-1f,-0x1.2d520ap-4f},{0x1.fe7ea8p-1f,-0x1.39d9f2p-4f},{0x1.fe5f3ap-1f,-0x1.466118p-4f},{0x1.fe3e92p-1f,-0x1.52e774p-4f},
{0x1.fe1cb0p-1f,-0x1.5f6d00p-4f},{0x1.fdf992p-1f,-0x1.6bf1b4p-4f},{0x1.fdd53ap-1f,-0x1.787586p-4f},{0x1.fdafa8p-1f,-0x1.84f872p-4f},
{0x1.fd88dap-1f,-0x1.917a6cp-4f},{0x1.fd60d2p-1f,-0x1.9dfb6ep-4f},{0x1.fd3792p-1f,-0x1.aa7b72p-4f},{0x1.fd0d16p-1f,-0x1.b6fa6ep-4f},
{0x1.fce160p-1f,-0x1.c3785cp-4f},{0x1.fcb470p-1f,-0x1.cff534p-4f},{0x1.fc8646p-1f,-0x1.dc70ecp-4f},{0x1.fc56e4p-1f,-0x1.e8eb80p-4f},
{0x1.fc2648p-1f,-0x1.f564e6p-4f},{0x1.fbf470p-1f,-0x1.00ee8ap-3f},{0x1.fbc162p-1f,-0x1.072a04p-3f},{0x1.fb8d18p-1f,-0x1.0d64dcp-3f},
{0x1.fb5798p-1f,-0x1.139f0cp-3f},{0x1.fb20dcp-1f,-0x1.19d894p-3f},{0x1.fae8e8p-1f,-0x1.20116ep-3f},{0x1.faafbcp-1f,-0x1.264994p-3f},
{0x1.fa7558p-1f,-0x1.2c8106p-3f},{0x1.fa39bap-1f,-0x1.32b7c0p-3f},{0x1.f9fce6p-1f,-0x1.38edbcp-3f},{0x1.f9bed8p-1f,-0x1.3f22f6p-3f},
{0x1.f97f92p-1f,-0x1.45576cp-3f},{0x1.f93f14p-1f,-0x1.4b8b18p-3f},{0x1.f8fd60p-1f,-0x1.51bdf8p-3f},{0x1.f8ba74p-1f,-0x1.57f008p-3f},
{0x1.f87650p-1f,-0x1.5e2144p-3f},{0x1.f830f4p-1f,-0x1.6451a8p-3f},{0x1.f7ea62p-1f,-0x1.6a8130p-3f},{0x1.f7a29ap-1f,-0x1.70afd8p-3f},
{0x1.f7599ap-1f,-0x1.76dd9ep-3f},{0x1.f70f64p-1f,-0x1.7d0a7cp-3f},{0x1.f6c3f8p-1f,-0x1.83366ep-3f},{0x1.f67756p-1f,-0x1.896172p-3f},
{0x1.f6297cp-1f,-0x1.8f8b84p-3f},{0x1.f5da6ep-1f,-0x1.95b49ep-3f},{0x1.f58a2cp-1f,-0x1.9bdcc0p-3f},{0x1.f538b2p-1f,-0x1.a203e2p-3f},
{0x1.f4e604p-1f,-0x1.a82a02p-3f},{0x1.f49220p-1f,-0x1.ae4f1ep-3f},{0x1.f43d08p-1f,-0x1.b4732ep-3f},{0x1.f3e6bcp-1f,-0x1.ba9634p-3f},
{0x1.f38f3ap-1f,-0x1.c0b826p-3f},{0x1.f33686p-1f,-0x1.c6d906p-3f},{0x1.f2dc9cp-1f,-0x1.ccf8ccp-3f},{0x1.f28180p-1f,-0x1.d31774p-3f},
{0x1.f22530p-1f,-0x1.d934fep-3f},{0x1.f1c7acp-1f,-0x1.df5164p-3f},{0x1.f168f6p-1f,-0x1.e56ca2p-3f},{0x1.f1090cp-1f,-0x1.eb86b4p-3f},
{0x1.f0a7f0p-1f,-0x1.f19f98p-3f},{0x1.f045a2p-1f,-0x1.f7b748p-3f},{0x1.efe220p-1f,-0x1.fdcdc2p-3f},{0x1.ef7d6ep-1f,-0x1.01f180p-2f},
{0x1.ef178ap-1f,-0x1.04fb80p-2f},{0x1.eeb074p-1f,-0x1.0804e0p-2f},{0x1.ee482ep-1f,-0x1.0b0d9cp-2f},{0x1.eddeb6p-1f,-0x1.0e15b4p-2f},
{0x1.ed740ep-1f,-0x1.111d26p-2f},{0x1.ed0836p-1f,-0x1.1423eep-2f},{0x1.ec9b2ep-1f,-0x1.172a0ep-2f},{0x1.ec2cf4p-1f,-0x1.1a2f80p-2f},
{0x1.ebbd8cp-1f,-0x1.1d3444p-2f},{0x1.eb4cf6p-1f,-0x1.203858p-2f},{0x1.eadb2ep-1f,-0x1.233bbap-2f},{0x1.ea683ap-1f,-0x1.263e6ap-2f},
{0x1.e9f416p-1f,-0x1.294062p-2f},{0x1.e97ec4p-1f,-0x1.2c41a4p-2f},{0x1.e90844p-1f,-0x1.2f422ep-2f},{0x1.e89096p-1f,-0x1.3241fcp-2f},
{0x1.e817bap-1f,-0x1.35410cp-2f},{0x1.e79db2p-1f,-0x1.383f5ep-2f},{0x1.e7227ep-1f,-0x1.3b3cf0p-2f},{0x1.e6a61cp-1f,-0x1.3e39bep-2f},
{0x1.e6288ep-1f,-0x1.4135cap-2f},{0x1.e5a9d6p-1f,-0x1.44310ep-2f},{0x1.e529f0p-1f,-0x1.472b8ap-2f},{0x1.e4a8e0p-1f,-0x1.4a253ep-2f},
{0x1.e426a4p-1f,-0x1.4d1e24p-2f},{0x1.e3a33ep-1f,-0x1.50163ep-2f},{0x1.e31eaep-1f,-0x1.530d88p-2f},{0x1.e298f4p-1f,-0x1.560402p-2f},
{0x1.e21210p-1f,-0x1.58f9a8p-2f},{0x1.e18a02p-1f,-0x1.5bee78p-2f},{0x1.e100ccp-1f,-0x1.5ee274p-2f},{0x1.e0766ep-1f,-0x1.61d596p-2f},
{0x1.dfeae6p-1f,-0x1.64c7dep-2f},{0x1.df5e36p-1f,-0x1.67b94ap-2f},{0x1.ded060p-1f,-0x1.6aa9d8p-2f},{0x1.de4160p-1f,-0x1.6d9986p-2f},
{0x1.ddb13cp-1f,-0x1.708854p-2f},{0x1.dd1ff0p-1f,-0x1.73763cp-2f},{0x1.dc8d7cp-1f,-0x1.766340p-2f},{0x1.dbf9e4p-1f,-0x1.794f5ep-2f},
{0x1.db6526p-1f,-0x1.7c3a94p-2f},{0x1.dacf42p-1f,-0x1.7f24dep-2f},{0x1.da383ap-1f,-0x1.820e3cp-2f},{0x1.d9a00ep-1f,-0x1.84f6aap-2f},
{0x1.d906bcp-1f,-0x1.87de2ap-2f},{0x1.d86c48p-1f,-0x1.8ac4b8p-2f},{0x1.d7d0b0p-1f,-0x1.8daa52p-2f},{0x1.d733f6p-1f,-0x1.908ef8p-2f},
{0x1.d69618p-1f,-0x1.9372a6p-2f},{0x1.d5f718p-1f,-0x1.96555cp-2f},{0x1.d556f6p-1f,-0x1.993716p-2f},{0x1.d4b5b2p-1f,-0x1.9c17d4p-2f},
{0x1.d4134ep-1f,-0x1.9ef794p-2f},{0x1.d36fc8p-1f,-0x1.a1d654p-2f},{0x1.d2cb22p-1f,-0x1.a4b412p-2f},{0x1.d2255cp-1f,-0x1.a790cep-2f},
{0x1.d17e78p-1f,-0x1.aa6c82p-2f},{0x1.d0d672p-1f,-0x1.ad4732p-2f},{0x1.d02d50p-1f,-0x1.b020d6p-2f},{0x1.cf830ep-1f,-0x1.b2f972p-2f},
{0x1.ced7b0p-1f,-0x1.b5d100p-2f},{0x1.ce2b32p-1f,-0x1.b8a782p-2f},{0x1.cd7d98p-1f,-0x1.bb7cf2p-2f},{0x1.cccee2p-1f,-0x1.be5152p-2f},
{0x1.cc1f10p-1f,-0x1.c1249ep-2f},{0x1.cb6e20p-1f,-0x1.c3f6d4p-2f},{0x1.cabc16p-1f,-0x1.c6c7f4p-2f},{0x1.ca08f2p-1f,-0x1.c997fcp-2f},
{0x1.c954b2p-1f,-0x1.cc66eap-2f},{0x1.c89f58p-1f,-0x1.cf34bap-2f},{0x1.c7e8e6p-1f,-0x1.d2016ep-2f},{0x1.c73158p-1f,-0x1.d4cd02p-2f},
{0x1.c678b4p-1f,-0x1.d79776p-2f},{0x1.c5bef6p-1f,-0x1.da60c6p-2f},{0x1.c50420p-1f,-0x1.dd28f2p-2f},{0x1.c44834p-1f,-0x1.dfeff6p-2f},
{0x1.c38b30p-1f,-0x1.e2b5d4p-2f},{0x1.c2cd14p-1f,-0x1.e57a86p-2f},{0x1.c20de4p-1f,-0x1.e83e0ep-2f},{0x1.c14d9ep-1f,-0x1.eb006ap-2f},
{0x1.c08c42p-1f,-0x1.edc196p-2f},{0x1.bfc9d2p-1f,-0x1.f08190p-2f},{0x1.bf064ep-1f,-0x1.f3405ap-2f},{0x1.be41b6p-1f,-0x1.f5fdeep-2f},
{0x1.bd7c0ap-1f,-0x1.f8ba4ep-2f},{0x1.bcb54cp-1f,-0x1.fb7576p-2f},{0x1.bbed7cp-1f,-0x1.fe2f64p-2f},{0x1.bb249ap-1f,-0x1.00740cp-1f},
{0x1.ba5aa6p-1f,-0x1.01cfc8p-1f},{0x1.b98fa2p-1f,-0x1.032ae6p-1f},{0x1.b8c38ep-1f,-0x1.048562p-1f},{0x1.b7f668p-1f,-0x1.05df3ep-1f},
{0x1.b72834p-1f,-0x1.07387ap-1f},{0x1.b658f2p-1f,-0x1.089112p-1f},{0x1.b588a0p-1f,-0x1.09e908p-1f},{0x1.b4b740p-1f,-0x1.0b4058p-1f},
{0x1.b3e4d4p-1f,-0x1.0c9704p-1f},{0x1.b3115ap-1f,-0x1.0ded0cp-1f},{0x1.b23cd4p-1f,-0x1.0f426cp-1f},{0x1.b16742p-1f,-0x1.109724p-1f},
{0x1.b090a6p-1f,-0x1.11eb36p-1f},{0x1.afb8fep-1f,-0x1.133e9cp-1f},{0x1.aee04cp-1f,-0x1.14915ap-1f},{0x1.ae0690p-1f,-0x1.15e36ep-1f},
{0x1.ad2bcap-1f,-0x1.1734d6p-1f},{0x1.ac4ffcp-1f,-0x1.188592p-1f},{0x1.ab7326p-1f,-0x1.19d5a0p-1f},{0x1.aa9548p-1f,-0x1.1b2502p-1f},
{0x1.a9b662p-1f,-0x1.1c73b4p-1f},{0x1.a8d676p-1f,-0x1.1dc1b6p-1f},{0x1.a7f586p-1f,-0x1.1f0f08p-1f},{0x1.a7138ep-1f,-0x1.205baap-1f},
{0x1.a63092p-1f,-0x1.21a79ap-1f},{0x1.a54c92p-1f,-0x1.22f2d6p-1f},{0x1.a4678cp-1f,-0x1.243d60p-1f},{0x1.a38184p-1f,-0x1.258734p-1f},
{0x1.a29a7ap-1f,-0x1.26d054p-1f},{0x1.a1b26ep-1f,-0x1.2818bep-1f},{0x1.a0c95ep-1f,-0x1.296072p-1f},{0x1.9fdf50p-1f,-0x1.2aa76ep-1f},
{0x1.9ef43ep-1f,-0x1.2bedb2p-1f},{0x1.9e082ep-1f,-0x1.2d333ep-1f},{0x1.9d1b20p-1f,-0x1.2e780ep-1f},{0x1.9c2d12p-1f,-0x1.2fbc24p-1f},
{0x1.9b3e04p-1f,-0x1.30ff80p-1f},{0x1.9a4dfap-1f,-0x1.32421ep-1f},{0x1.995cf2p-1f,-0x1.338400p-1f},{0x1.986af0p-1f,-0x1.34c526p-1f},
{0x1.9777f0p-1f,-0x1.36058cp-1f},{0x1.9683f4p-1f,-0x1.374532p-1f},{0x1.958efep-1f,-0x1.388418p-1f},{0x1.94990ep-1f,-0x1.39c23ep-1f},
{0x1.93a224p-1f,-0x1.3affa2p-1f},{0x1.92aa42p-1f,-0x1.3c3c44p-1f},{0x1.91b166p-1f,-0x1.3d7824p-1f},{0x1.90b794p-1f,-0x1.3eb33ep-1f},
{0x1.8fbccap-1f,-0x1.3fed96p-1f},{0x1.8ec10ap-1f,-0x1.412726p-1f},{0x1.8dc454p-1f,-0x1.425ff2p-1f},{0x1.8cc6a8p-1f,-0x1.4397f6p-1f},
{0x1.8bc806p-1f,-0x1.44cf32p-1f},{0x1.8ac872p-1f,-0x1.4605a6p-1f},{0x1.89c7eap-1f,-0x1.473b52p-1f},{0x1.88c66ep-1f,-0x1.487034p-1f},
{0x1.87c400p-1f,-0x1.49a44ap-1f},{0x1.86c0a2p-1f,-0x1.4ad796p-1f},{0x1.85bc52p-1f,-0x1.4c0a14p-1f},{0x1.84b712p-1f,-0x1.4d3bc6p-1f},
{0x1.83b0e0p-1f,-0x1.4e6cacp-1f},{0x1.82a9c2p-1f,-0x1.4f9cc2p-1f},{0x1.81a1b4p-1f,-0x1.50cc0ap-1f},{0x1.8098b8p-1f,-0x1.51fa82p-1f},
{0x1.7f8ecep-1f,-0x1.53282ap-1f},{0x1.7e83f8p-1f,-0x1.545500p-1f},{0x1.7d7836p-1f,-0x1.558104p-1f},{0x1.7c6b8ap-1f,-0x1.56ac36p-1f},
{0x1.7b5df2p-1f,-0x1.57d694p-1f},{0x1.7a4f70p-1f,-0x1.59001ep-1f},{0x1.794006p-1f,-0x1.5a28d2p-1f},{0x1.782fb2p-1f,-0x1.5b50b2p-1f},
{0x1.771e76p-1f,-0x1.5c77bcp-1f},{0x1.760c52p-1f,-0x1.5d9deep-1f},{0x1.74f948p-1f,-0x1.5ec34ap-1f},{0x1.73e558p-1f,-0x1.5fe7ccp-1f},
{0x1.72d084p-1f,-0x1.610b76p-1f},{0x1.71bacap-1f,-0x1.622e44p-1f},{0x1.70a42cp-1f,-0x1.63503ap-1f},{0x1.6f8caap-1f,-0x1.647154p-1f},
{0x1.6e7446p-1f,-0x1.659192p-1f},{0x1.6d5afep-1f,-0x1.66b0f4p-1f},{0x1.6c40d8p-1f,-0x1.67cf78p-1f},{0x1.6b25cep-1f,-0x1.68ed1ep-1f},
};


#define DEV __device__ __forceinline__
#define AINL __attribute__((always_inline))
#define WB() __builtin_amdgcn_wave_barrier()
template <int V> struct IC { static constexpr int v = V; };
template <int I, int N, class F> DEV void sfor(F&& f) { if constexpr (I < N) { f(IC<I>{}); sfor<I + 1, N>(f); } }

DEV cf mk(float x, float y) { cf r; r.x = x; r.y = y; return r; }
DEV cf add_mi(cf a, cf b) {
  cf d; asm("v_pk_add_f32 %0, %2, %1 op_sel:[1,0] op_sel_hi:[0,1] neg_hi:[1,0]" : "=v"(d) : "v"(a), "v"(b)); return d;
}
DEV cf add_pi(cf a, cf b) {
  cf d; asm("v_pk_add_f32 %0, %2, %1 op_sel:[1,0] op_sel_hi:[0,1] neg_lo:[1,0]" : "=v"(d) : "v"(a), "v"(b)); return d;
}
DEV cf cj_pi(cf a, cf b) {
  cf d; asm("v_pk_add_f32 %0, %2, %1 op_sel:[1,0] op_sel_hi:[0,1] neg_hi:[0,1]" : "=v"(d) : "v"(a), "v"(b)); return d;
}
DEV cf add_cj(cf a, cf b) {
  cf d; asm("v_pk_add_f32 %0, %1, %2 neg_hi:[0,1]" : "=v"(d) : "v"(a), "v"(b)); return d;
}
DEV cf sub_cj(cf a, cf b) {
  cf d; asm("v_pk_add_f32 %0, %1, %2 neg_lo:[0,1]" : "=v"(d) : "v"(a), "v"(b)); return d;
}
DEV cf cmul(cf a, cf w) {
  cf d;
  asm("v_pk_mul_f32 %0, %1, %2 op_sel_hi:[0,1]\n\tv_pk_fma_f32 %0, %1, %2, %0 op_sel:[1,1,0] op_sel_hi:[1,0,1] neg_lo:[0,1,0]" : "=&v"(d) : "v"(a), "v"(w));
  return d;
}
DEV cf cmulc(cf a, cf w) {
  cf d;
  asm("v_pk_mul_f32 %0, %1, %2 op_sel_hi:[0,1] neg_hi:[0,1]\n\tv_pk_fma_f32 %0, %1, %2, %0 op_sel:[1,1,0] op_sel_hi:[1,0,1]" : "=&v"(d) : "v"(a), "v"(w));
  return d;
}
DEV cf cmul_s(cf a, cf w) {
  cf d;
  asm("v_pk_mul_f32 %0, %1, %2 op_sel_hi:[0,1]\n\tv_pk_fma_f32 %0, %1, %2, %0 op_sel:[1,1,0] op_sel_hi:[1,0,1] neg_lo:[0,1,0]" : "=&v"(d) : "v"(a), "s"(w));
  return d;
}
DEV cf cmulc_s(cf a, cf w) {
  cf d;
  asm("v_pk_mul_f32 %0, %1, %2 op_sel_hi:[0,1] neg_hi:[0,1]\n\tv_pk_fma_f32 %0, %1, %2, %0 op_sel:[1,1,0] op_sel_hi:[1,0,1]" : "=&v"(d) : "v"(a), "s"(w));
  return d;
}
template <bool INV> DEV cf ctw_s(cf a, cf w) { return INV ? cmulc_s(a, w) : cmul_s(a, w); }
template <bool INV> DEV cf add_r(cf a, cf b) { return INV ? add_pi(a, b) : add_mi(a, b); }
template <bool INV> DEV cf sub_r(cf a, cf b) { return INV ? add_mi(a, b) : add_pi(a, b); }

DEV void sto8(uint32_t* p, uint32_t a, uint32_t b) {
  const u32x2 v = {a, b};
  __builtin_nontemporal_store(v, (u32x2*)p);
}
typedef const volatile __attribute__((address_space(3))) cf* lds_cvp;
DEV cf LR(const cf* p) { return *(lds_cvp)p; }
DEV float bf2f(uint32_t bits16) { return __uint_as_float(bits16 << 16); }
DEV float bflo(uint32_t w) { return __uint_as_float(w << 16); }
DEV float bfhi(uint32_t w) { return __uint_as_float(w & 0xffff0000u); }
DEV uint32_t f2bf(float f) {
  __hip_bfloat16 h = __float2bfloat16(f);
  return (uint32_t)__bfloat16_as_ushort(h);
}
DEV float bfround(float f) { return bf2f(f2bf(f)); }
DEV uint32_t cvt_pk(float a, float b) {
  return __builtin_bit_cast(uint32_t, __builtin_convertvector(mk(a, b), bf16x2));
}
DEV cf unpk(uint32_t w) { return mk(bflo(w), bfhi(w)); }

DEV cf cmul4(cf a, cf w) {
  float x, y;
  asm("v_mul_f32 %0, %2, %4\n\tv_mul_f32 %1, %2, %5\n\tv_fma_f32 %0, -%3, %5, %0\n\tv_fma_f32 %1, %3, %4, %1" : "=&v"(x), "=&v"(y) : "v"(a.x), "v"(a.y), "v"(w.x), "v"(w.y));
  return mk(x, y);
}
DEV cf cmulc4(cf a, cf w) {
  float x, y;
  asm("v_mul_f32 %0, %2, %4\n\tv_mul_f32 %1, %3, %4\n\tv_fma_f32 %0, %3, %5, %0\n\tv_fma_f32 %1, -%2, %5, %1" : "=&v"(x), "=&v"(y) : "v"(a.x), "v"(a.y), "v"(w.x), "v"(w.y));
  return mk(x, y);
}
DEV cf cmul4_s(cf a, cf w) {
  float x, y;
  asm("v_mul_f32 %0, %4, %2\n\tv_mul_f32 %1, %5, %2\n\tv_fma_f32 %0, -%3, %5, %0\n\tv_fma_f32 %1, %3, %4, %1" : "=&v"(x), "=&v"(y) : "v"(a.x), "v"(a.y), "s"(w.x), "s"(w.y));
  return mk(x, y);
}
DEV cf cfma_cj4(cf acc, cf m, cf b) {
  float x = acc.x, y = acc.y;
  asm("v_fma_f32 %0, %2, %4, %0\n\tv_fma_f32 %1, %2, %5, %1\n\tv_fma_f32 %0, %3, %5, %0\n\tv_fma_f32 %1, -%3, %4, %1" : "+v"(x), "+v"(y) : "v"(m.x), "v"(m.y), "v"(b.x), "v"(b.y));
  return mk(x, y);
}
template <bool S> DEV cf CM(cf a, cf w) { return S ? cmul4(a, w) : cmul(a, w); }
template <bool S> DEV cf CMC(cf a, cf w) { return S ? cmulc4(a, w) : cmulc(a, w); }
template <bool S, bool INV> DEV cf CTW(cf a, cf w) { return S ? cmul4_s(a, INV ? mk(w.x, -w.y) : w) : ctw_s<INV>(a, w); }
DEV void bf2s(cf& a, cf& b) {
  float x, y, bx = b.x, by = b.y;
  asm("v_add_f32 %0, %4, %2\n\tv_add_f32 %1, %5, %3\n\tv_sub_f32 %2, %4, %2\n\tv_sub_f32 %3, %5, %3" : "=&v"(x), "=&v"(y), "+v"(bx), "+v"(by) : "v"(a.x), "v"(a.y));
  a = mk(x, y); b = mk(bx, by);
}
DEV void bf2rs(cf& a, cf& b) {
  float x, y, bx = b.x, by = b.y;
  asm("v_add_f32 %0, %4, %3\n\tv_sub_f32 %1, %5, %2\n\tv_sub_f32 %3, %4, %3\n\tv_add_f32 %2, %5, %2" : "=&v"(x), "=&v"(y), "+v"(bx), "+v"(by) : "v"(a.x), "v"(a.y));
  a = mk(x, y); b = mk(by, bx);
}
template <bool INV> DEV void bfr(cf& a, cf& b) { bf2rs(a, b); if (INV) { cf t = a; a = b; b = t; } }
template <bool INV, bool R2> DEV void r4s(cf& a0, cf& a1, cf& a2, cf& a3) {
  if (R2) bfr<INV>(a0, a2); else bf2s(a0, a2);
  bf2s(a1, a3);
  bf2s(a0, a1);
  bfr<INV>(a2, a3);
  cf t = a1; a1 = a2; a2 = t;
}
template <bool INV> DEV void r4(cf& a0, cf& a1, cf& a2, cf& a3) {
  cf t0 = a0 + a2, t1 = a0 - a2, t2 = a1 + a3, t3 = a1 - a3;
  a0 = t0 + t2; a2 = t0 - t2; a1 = add_r<INV>(t1, t3); a3 = sub_r<INV>(t1, t3);
}
template <bool INV> DEV void r4_r2(cf& a0, cf& a1, cf& a2, cf& a3) {
  cf t0 = add_r<INV>(a0, a2), t1 = sub_r<INV>(a0, a2), t2 = a1 + a3, t3 = a1 - a3;
  a0 = t0 + t2; a2 = t0 - t2; a1 = add_r<INV>(t1, t3); a3 = sub_r<INV>(t1, t3);
}
#define RSQ2 0.70710678118654752440f
#define C16_1 0.92387953251128675613f
#define S16_1 0.38268343236508977173f
template <bool INV> DEV void r8(cf* v) {
  r4<INV>(v[0], v[2], v[4], v[6]);
  r4<INV>(v[1], v[3], v[5], v[7]);
  cf e0 = v[0], e1 = v[2], e2 = v[4], e3 = v[6];
  cf o0 = v[1], o1 = ctw_s<INV>(v[3], mk(RSQ2, -RSQ2)), o2 = v[5], o3 = ctw_s<INV>(v[7], mk(-RSQ2, -RSQ2));
  v[0] = e0 + o0; v[4] = e0 - o0;
  v[1] = e1 + o1; v[5] = e1 - o1;
  v[2] = add_r<INV>(e2, o2); v[6] = sub_r<INV>(e2, o2);
  v[3] = e3 + o3; v[7] = e3 - o3;
}
template <bool INV, bool S = false> DEV void r16(cf* v) {
#define R4(a, b, c, d) do { if (S) r4s<INV, false>(a, b, c, d); else r4<INV>(a, b, c, d); } while (0)
  R4(v[0], v[4], v[8], v[12]);
  R4(v[1], v[5], v[9], v[13]);
  R4(v[2], v[6], v[10], v[14]);
  R4(v[3], v[7], v[11], v[15]);
  const cf w1 = mk(C16_1, -S16_1), w3 = mk(S16_1, -C16_1), w9 = mk(-C16_1, S16_1);
  const cf w2 = mk(RSQ2, -RSQ2), w6 = mk(-RSQ2, -RSQ2);
  v[5] = CTW<S, INV>(v[5], w1);
  v[6] = CTW<S, INV>(v[6], w2);
  v[7] = CTW<S, INV>(v[7], w3);
  v[9] = CTW<S, INV>(v[9], w2);
  v[11] = CTW<S, INV>(v[11], w6);
  v[13] = CTW<S, INV>(v[13], w3);
  v[14] = CTW<S, INV>(v[14], w6);
  v[15] = CTW<S, INV>(v[15], w9);
  R4(v[0], v[1], v[2], v[3]);
  R4(v[4], v[5], v[6], v[7]);
  if (S) r4s<INV, true>(v[8], v[9], v[10], v[11]); else r4_r2<INV>(v[8], v[9], v[10], v[11]);
  R4(v[12], v[13], v[14], v[15]);
#undef R4
  cf t;
  t = v[1]; v[1] = v[4]; v[4] = t;
  t = v[2]; v[2] = v[8]; v[8] = t;
  t = v[3]; v[3] = v[12]; v[12] = t;
  t = v[6]; v[6] = v[9]; v[9] = t;
  t = v[7]; v[7] = v[13]; v[13] = t;
  t = v[11]; v[11] = v[14]; v[14] = t;
}

template <bool S = false> DEV void pow_tree15(cf w1, cf* w) {
  w[1] = w1;
  w[2] = CM<S>(w1, w1);
  w[3] = CM<S>(w[2], w1);
  w[4] = CM<S>(w[2], w[2]);
  w[5] = CM<S>(w[4], w1);
  w[6] = CM<S>(w[3], w[3]);
  w[7] = CM<S>(w[4], w[3]);
  w[8] = CM<S>(w[4], w[4]);
  w[9] = CM<S>(w[8], w1);
  w[10] = CM<S>(w[5], w[5]);
  w[11] = CM<S>(w[8], w[3]);
  w[12] = CM<S>(w[6], w[6]);
  w[13] = CM<S>(w[8], w[5]);
  w[14] = CM<S>(w[7], w[7]);
  w[15] = CM<S>(w[8], w[7]);
}
DEV void pow_tree7(cf w1, cf* w) {
  w[1] = w1;
  w[2] = cmul(w1, w1);
  w[3] = cmul(w[2], w1);
  w[4] = cmul(w[2], w[2]);
  w[5] = cmul(w[4], w1);
  w[6] = cmul(w[3], w[3]);
  w[7] = cmul(w[4], w[3]);
}

#define T_GSTR 4112
#define T_BYTES (256u * T_GSTR * 8u)
#define SY_B 0
#define SY_DN 64
#define SY_RL (SY_DN + 512)
#define SY_PQ (SY_RL + 1024)
#define SPIN_MAX (1 << 16)
DEV uint32_t ld_c(const uint32_t* q) { return __hip_atomic_load(q, __ATOMIC_RELAXED, __HIP_MEMORY_SCOPE_AGENT); }
DEV void st_c(uint32_t* q, uint32_t v) { __hip_atomic_store(q, v, __ATOMIC_RELAXED, __HIP_MEMORY_SCOPE_AGENT); }
DEV uint64_t ld_c2(const uint32_t* q) { return __hip_atomic_load((const uint64_t*)q, __ATOMIC_RELAXED, __HIP_MEMORY_SCOPE_AGENT); }
DEV void st_c2(uint32_t* q, uint32_t a, uint32_t b) { __hip_atomic_store((uint64_t*)q, (uint64_t)a | ((uint64_t)b << 32), __ATOMIC_RELAXED, __HIP_MEMORY_SCOPE_AGENT); }
#define LDC(d, q) asm volatile("global_load_dword %0, %1, off sc1" : "=v"(d) : "v"(q) : "memory")
struct Params {
  const uint16_t* u; const uint16_t* a_re; const uint16_t* a_im; const uint16_t* b_re; const uint16_t* b_im;
  const uint16_t* c_re; const uint16_t* c_im; const uint16_t* log_dt; const uint16_t* dskip;
  const uint16_t* x; const uint16_t* w_r; const uint16_t* w_i;
  uint16_t* out; cf* T; uint32_t* cnt;
};

#define B_SW 1096
#define B_RS 136
DEV void pow_tree7s(cf w1, cf c, cf* o) {
  const cf w2 = cmul(w1, w1), w4 = cmul(w2, w2);
  o[0] = c; o[1] = cmul(c, w1); o[2] = cmul(c, w2); o[3] = cmul(o[1], w2);
  o[4] = cmul(c, w4); o[5] = cmul(o[1], w4); o[6] = cmul(o[2], w4); o[7] = cmul(o[3], w4);
}
DEV void b_coltw(int g, int w, cf& cw, cf& c4) {
  const cf c1 = TW[128 + g], c2 = cmul(c1, c1), c3 = cmul(c2, c1);
  c4 = cmul(c2, c2);
  cw = w == 0 ? mk(1.f, 0.f) : w == 1 ? c1 : w == 2 ? c2 : c3;
}
template <int O> DEV void ldg8(cf& d, const cf* q) { asm volatile("global_load_dwordx2 %0, %1, off offset:%2 sc1" : "+v"(d) : "v"(q), "n"(O) : "memory"); }
DEV cf tag1(cf v) { return mk(__uint_as_float(__float_as_uint(v.x) | 1u), v.y); }
DEV cf tag0(cf v) { return mk(__uint_as_float(__float_as_uint(v.x) & ~1u), v.y); }
template <class F> DEV void fetch16(cf* v, F&& ld) {
  sfor<0, 16>(ld);
#pragma unroll 1
  for (int it = 0; it < SPIN_MAX; ++it) {
    asm volatile("s_waitcnt vmcnt(0)" : "+v"(v[0]), "+v"(v[1]), "+v"(v[2]), "+v"(v[3]), "+v"(v[4]), "+v"(v[5]), "+v"(v[6]), "+v"(v[7]),
                 "+v"(v[8]), "+v"(v[9]), "+v"(v[10]), "+v"(v[11]), "+v"(v[12]), "+v"(v[13]), "+v"(v[14]), "+v"(v[15]) :: "memory");
    uint32_t m = 0;
#pragma unroll
    for (int j = 0; j < 16; ++j) m |= __float_as_uint(v[j].x);
    if (!__ballot(m & 1u)) break;
    sfor<0, 16>([&](auto jc) AINL { if (__float_as_uint(v[decltype(jc)::v].x) & 1u) ld(jc); });
  }
  asm volatile("s_waitcnt vmcnt(0)" : "+v"(v[0]), "+v"(v[1]), "+v"(v[2]), "+v"(v[3]), "+v"(v[4]), "+v"(v[5]), "+v"(v[6]), "+v"(v[7]),
               "+v"(v[8]), "+v"(v[9]), "+v"(v[10]), "+v"(v[11]), "+v"(v[12]), "+v"(v[13]), "+v"(v[14]), "+v"(v[15]) :: "memory");
}
struct BT { cf c4, ta[8], tb[8], tw[16]; };
template <int O> DEV void st8o(cf* q, cf v) { asm volatile("global_store_dwordx2 %0, %1, off offset:%2 sc1" :: "v"(q), "v"(v), "n"(O) : "memory"); }
template <class F> DEV void role_b1(const Params& p, int g, cf* lds, BT& bt, cf& tw1, F&& hook) {
  const int tid = threadIdx.x, w = tid >> 6, t = tid & 63;
  const int SN = 130;
  cf* buf = lds + w * B_SW;
  const int ka = t >> 3, n0 = t & 7;
  cf c4;
  {
    f32x4 wab;
    cf wC, cw;
    asm volatile("global_load_dwordx4 %0, %1, off" : "=v"(wab) : "v"(TW + 2 * t) : "memory");
    asm volatile("global_load_dwordx2 %0, %1, off" : "=v"(wC) : "v"(TW + 8 * n0) : "memory");
    asm volatile("global_load_dwordx2 %0, %1, off" : "=v"(tw1) : "v"(TW + 4 * (tid >> 4)) : "memory");
    const uint2* xr = (const uint2*)(p.x + (size_t)g * 1024) + tid;
    u32x2 r[8];
    hook(IC<0>{});
#pragma unroll
    for (int s0 = 0; s0 < 8; ++s0) asm volatile("global_load_dwordx2 %0, %1, off" : "=v"(r[s0]) : "v"(xr + s0 * 65536) : "memory");
    hook(IC<1>{});
    b_coltw(g, w, cw, c4);
    asm volatile("s_waitcnt vmcnt(9)" : "+v"(wab), "+v"(wC) :: "memory");
    const cf cs = cw * (w ? 1.0f / 1024.0f : 1.0f / 2048.0f);
    pow_tree7s(mk(wab[0], wab[1]), cs, bt.ta); pow_tree7s(mk(wab[2], wab[3]), cs, bt.tb);
    pow_tree15(wC, bt.tw);
    bt.c4 = c4;
    asm volatile("" : "+v"(bt.ta[0]), "+v"(bt.ta[1]), "+v"(bt.ta[2]), "+v"(bt.ta[3]), "+v"(bt.ta[4]), "+v"(bt.ta[5]), "+v"(bt.ta[6]), "+v"(bt.ta[7]),
                 "+v"(bt.tb[0]), "+v"(bt.tb[1]), "+v"(bt.tb[2]), "+v"(bt.tb[3]), "+v"(bt.tb[4]), "+v"(bt.tb[5]), "+v"(bt.tb[6]), "+v"(bt.tb[7]));
    asm volatile("" : "+v"(bt.tw[1]), "+v"(bt.tw[2]), "+v"(bt.tw[3]), "+v"(bt.tw[4]), "+v"(bt.tw[5]), "+v"(bt.tw[6]), "+v"(bt.tw[7]), "+v"(bt.tw[8]),
                 "+v"(bt.tw[9]), "+v"(bt.tw[10]), "+v"(bt.tw[11]), "+v"(bt.tw[12]), "+v"(bt.tw[13]), "+v"(bt.tw[14]), "+v"(bt.tw[15]));
    asm volatile("s_waitcnt vmcnt(1)" : "+v"(r[0]), "+v"(r[1]), "+v"(r[2]), "+v"(r[3]), "+v"(r[4]), "+v"(r[5]), "+v"(r[6]), "+v"(r[7]),
                 "+v"(tw1) :: "memory");
#pragma unroll
    for (int hh = 0; hh < 2; ++hh) {
      cf x[8];
#pragma unroll
      for (int s0 = 0; s0 < 8; ++s0) x[s0] = unpk(r[s0][hh]);
      const cf a0 = x[0] + x[4], a1 = x[0] - x[4], a2 = x[2] + x[6], a3 = x[2] - x[6];
      const cf b0 = x[1] + x[5], b1 = x[1] - x[5], b2 = x[3] + x[7], b3 = x[3] - x[7];
      const cf se = a0 + a2, so = b0 + b2, dm = b1 - b3, dp = b1 + b3;
      cf* o = lds + 4 * tid + 2 * hh;
      *(float4*)(o) = make_float4(se.x + so.x, se.x - so.x, se.y + so.y, se.y - so.y);
      *(float4*)(o + B_SW) = make_float4(fmaf(RSQ2, dm.x, a1.x), fmaf(-RSQ2, dp.x, -a3.x), fmaf(RSQ2, dm.y, a1.y), fmaf(-RSQ2, dp.y, -a3.y));
      *(float4*)(o + 2 * B_SW) = make_float4(a0.x - a2.x, b2.x - b0.x, a0.y - a2.y, b2.y - b0.y);
      *(float4*)(o + 3 * B_SW) = make_float4(fmaf(-RSQ2, dm.x, a1.x), fmaf(-RSQ2, dp.x, a3.x), fmaf(-RSQ2, dm.y, a1.y), fmaf(-RSQ2, dp.y, a3.y));
    }
  }
  __syncthreads();
  {
    cf v0[8], v1[8];
#pragma unroll
    for (int j = 0; j < 8; ++j) {
      const float4 f = *(const float4*)(buf + 2 * t + 128 * j);
      v0[j] = mk(f.x, f.y); v1[j] = mk(f.z, f.w);
    }
    r8<false>(v0); r8<false>(v1);
    WB();
#pragma unroll
    for (int q = 0; q < 8; ++q) {
      const cf x0 = cmul(v0[q], bt.ta[q]), x1 = cmul(v1[q], bt.tb[q]);
      *(float4*)(buf + q * B_RS + 2 * t) = make_float4(x0.x, x0.y, x1.x, x1.y);
    }
  }
  WB();
  {
    cf v[16];
#pragma unroll
    for (int j = 0; j < 16; ++j) v[j] = LR(buf + ka * B_RS + n0 + 8 * j);
    r16<false>(v);
    WB();
#pragma unroll
    for (int kb = 0; kb < 16; ++kb) buf[n0 * SN + ka + 8 * kb] = kb ? cmul(v[kb], bt.tw[kb]) : v[kb];
  }
  WB();
  hook(IC<2>{});
  {
    const int ma = t, mb = t ? 128 - t : 64;
    cf a[8], b[8];
#pragma unroll
    for (int n = 0; n < 8; ++n) { a[n] = LR(buf + n * SN + ma); b[n] = LR(buf + n * SN + mb); }
    r8<false>(a); r8<false>(b);
    cf* Tg = p.T + (size_t)g * T_GSTR + 1024 * w;
    const cf hc = mk(c4.y, -c4.x);
    cf am[4], bm[4];
#pragma unroll
    for (int kc = 0; kc < 4; ++kc) { am[kc] = t ? b[7 - kc] : a[kc ? 8 - kc : 4]; bm[kc] = t ? a[7 - kc] : b[7 - kc]; }
    cf* q0 = Tg + ma; cf* q1 = Tg + 512 + ma; cf* q2 = Tg + mb; cf* q3 = Tg + 512 + mb;
#define B1_OUT(qz, qm, kc, Z, Zm) do { const cf z_ = (Z), m_ = (Zm); \
      st8o<1024 * (kc)>(qz, tag1(W0 ? add_cj(z_, m_) : z_)); st8o<1024 * (kc)>(qm, tag1(W0 ? cmul(sub_cj(z_, m_), hc) : mk(m_.x, -m_.y))); } while (0)
    auto fin = [&](auto wc) AINL {
      constexpr bool W0 = decltype(wc)::v;
      {
        const cf o1 = add_cj(a[0], am[0]), o2 = cmul(sub_cj(a[0], am[0]), hc);
        const cf p1 = mk(a[0].x + a[0].x, a[4].x + a[4].x), p2 = cmul(mk(a[0].y + a[0].y, a[4].y + a[4].y), c4);
        st8o<0>(q0, tag1(W0 ? (t ? o1 : p1) : a[0])); st8o<0>(q1, tag1(W0 ? (t ? o2 : p2) : mk(am[0].x, -am[0].y)));
      }
      B1_OUT(q2, q3, 0, b[0], bm[0]);
      B1_OUT(q0, q1, 1, a[1], am[1]); B1_OUT(q2, q3, 1, b[1], bm[1]);
      B1_OUT(q0, q1, 2, a[2], am[2]); B1_OUT(q2, q3, 2, b[2], bm[2]);
      B1_OUT(q0, q1, 3, a[3], am[3]); B1_OUT(q2, q3, 3, b[3], bm[3]);
    };
    if (__builtin_amdgcn_readfirstlane(w) == 0) fin(IC<1>{}); else fin(IC<0>{});
#undef B1_OUT
  }
}

DEV void role_b3(const Params& p, int g, cf* lds, const BT& bt) {
  const int tid = threadIdx.x, w = tid >> 6, t = tid & 63;
  const int SN = 132;
  const int ka = t >> 3, n0 = t & 7;
  const cf c4 = bt.c4;
  cf* buf = lds + w * B_SW;
  {
    const int ma = t, mb = t ? 128 - t : 64;
    const cf* Tg = p.T + (size_t)g * T_GSTR + 1024 * w;
    cf qv[16];
    const cf* pp[4] = {Tg + ma, Tg + 512 + ma, Tg + mb, Tg + 512 + mb};
    fetch16(qv, [&](auto jc) AINL {
      constexpr int j = decltype(jc)::v;
      ldg8<1024 * (j >> 2)>(qv[j], pp[j & 3]);
    });
    const cf qa[4][2] = {{qv[0], qv[1]}, {qv[4], qv[5]}, {qv[8], qv[9]}, {qv[12], qv[13]}};
    const cf qb[4][2] = {{qv[2], qv[3]}, {qv[6], qv[7]}, {qv[10], qv[11]}, {qv[14], qv[15]}};
    const cf hc = mk(c4.x, -c4.y);
    cf a[8], b[8];
#define B3_IN(q, ZP, ZM) do { const cf s0_ = (q)[0], s1_ = W0 ? cmul((q)[1], hc) : (q)[1]; \
      ZP = W0 ? add_pi(s0_, s1_) : s0_; ZM = W0 ? cj_pi(s0_, s1_) : mk(s1_.x, -s1_.y); } while (0)
    cf am[4], bm[4];
    auto ini = [&](auto wc) AINL {
      constexpr bool W0 = decltype(wc)::v;
#pragma unroll
      for (int kc = 0; kc < 4; ++kc) { B3_IN(qa[kc], a[kc], am[kc]); B3_IN(qb[kc], b[kc], bm[kc]); }
      if (W0) {
        const cf s0 = qa[0][0], sb = cmul(qa[0][1], hc);
        a[0] = t ? a[0] : mk(s0.x, sb.x);
        am[0] = t ? am[0] : mk(s0.y, sb.y);
      }
    };
    if (__builtin_amdgcn_readfirstlane(w) == 0) ini(IC<1>{}); else ini(IC<0>{});
#pragma unroll
    for (int kc = 0; kc < 4; ++kc) { a[7 - kc] = t ? bm[kc] : am[(kc + 1) & 3]; b[7 - kc] = t ? am[kc] : bm[kc]; }
#undef B3_IN
    r8<true>(a); r8<true>(b);
#pragma unroll
    for (int n = 0; n < 8; ++n) { buf[n * SN + ma] = a[n]; buf[n * SN + mb] = b[n]; }
  }
  WB();
  {
    cf v[16];
#pragma unroll
    for (int kb = 0; kb < 16; ++kb) {
      const cf x = LR(buf + n0 * SN + ka + 8 * kb);
      v[kb] = kb ? cmulc(x, bt.tw[kb]) : x;
    }
    r16<true>(v);
    WB();
#pragma unroll
    for (int j = 0; j < 16; ++j) buf[ka * B_RS + n0 + 8 * j] = v[j];
  }
  WB();
  {
    cf v0[8], v1[8];
#pragma unroll
    for (int q = 0; q < 8; ++q) {
      float4 f = *(const float4*)(buf + q * B_RS + 2 * t);
      v0[q] = cmulc(mk(f.x, f.y), bt.ta[q]);
      v1[q] = cmulc(mk(f.z, f.w), bt.tb[q]);
    }
    r8<true>(v0); r8<true>(v1);
    WB();
#pragma unroll
    for (int j = 0; j < 8; ++j) *(float4*)(buf + 2 * t + 128 * j) = make_float4(v0[j].x, v0[j].y, v1[j].x, v1[j].y);
  }
  __syncthreads();
  {
    float o[8][4];
#pragma unroll
    for (int hh = 0; hh < 2; ++hh) {
      const cf* src = lds + 4 * tid + 2 * hh;
      const float4 f0 = *(const float4*)src, f1 = *(const float4*)(src + B_SW), f2 = *(const float4*)(src + 2 * B_SW), f3 = *(const float4*)(src + 3 * B_SW);
#pragma unroll
      for (int i = 0; i < 2; ++i) {
        const float y0 = i ? f0.z : f0.x, y4 = i ? f0.w : f0.y, r1 = i ? f1.z : f1.x, i1 = i ? f1.w : f1.y;
        const float r2 = i ? f2.z : f2.x, i2 = i ? f2.w : f2.y, r3 = i ? f3.z : f3.x, i3 = i ? f3.w : f3.y;
        const float e0 = y0 + y4, e1 = y0 - y4;
        const float p0 = e0 + r2, p1 = e0 - r2, p2 = e1 - i2, p3 = e1 + i2;
        const float rs = r1 + r3, rd = r1 - r3, id = i3 - i1, is = i1 + i3;
        const float u0 = rd - is, u1 = rd + is;
        const int c = 2 * hh + i;
        o[0][c] = p0 + rs; o[4][c] = p0 - rs; o[2][c] = p1 + id; o[6][c] = p1 - id;
        o[1][c] = fmaf(RSQ2, u0, p2); o[5][c] = fmaf(-RSQ2, u0, p2); o[3][c] = fmaf(-RSQ2, u1, p3); o[7][c] = fmaf(RSQ2, u1, p3);
      }
    }
    uint32_t* op = (uint32_t*)(p.out + (size_t)g * 2048 + 1024) + 2 * tid;
#pragma unroll
    for (int s0 = 0; s0 < 8; ++s0) sto8(op + (size_t)s0 * 256 * 1024, cvt_pk(o[s0][0], o[s0][1]), cvt_pk(o[s0][2], o[s0][3]));
  }
}

#define C_RS 272
DEV void b2_wload(const Params& p, int c, u32x4* wv) {
  const int tid = threadIdx.x, lane = tid & 63, slot = c & 7;
  const int res = slot == 1 ? 4 : (slot & 1) ? -(slot >> 1) : (slot >> 1);
  const int row = 128 * (lane >> 3) + 8 * (4 * (tid >> 6) + ((lane >> 1) & 3)) + res;
  const int o = 2 * (c & ~7) + 8 * (lane & 1);
#pragma unroll
  for (int n = 0; n < 4; ++n) {
    const uint16_t* q = ((n & 2) ? p.w_i : p.w_r) + (size_t)((row + 1024 * (n & 1)) & 2047) * 513 + o;
    asm volatile("global_load_dwordx4 %0, %1, off" : "=v"(wv[n]) : "v"(q) : "memory");
  }
}
DEV void role_b2(const Params& p, int c, cf* buf, char* wl, const cf* tw) {
  const int tid = threadIdx.x, ka = tid >> 4, e = tid & 15;
  const int slot = c & 7, k = 2 * (c & ~7) + e;
  const __amdgpu_buffer_rsrc_t rt = __builtin_amdgcn_make_buffer_rsrc((void*)p.T, 0, T_BYTES, 0x00020000);
  const int col = (ka * T_GSTR + 512 * slot + k) * 8;
  const f32x4* pq = (const f32x4*)(buf + 16 * C_RS + 256) + 16 * ka;
  u32x4 wv[4];
  cf wf[16];
  const bool sp = k == 0;
  {
    cf v[16];
    f32x4 x;
    if (c < 8) asm volatile("global_load_dwordx4 %0, %1, off sc1" : "=v"(x) : "v"(p.cnt + SY_PQ + 1024 * c + 4 * tid) : "memory");
#pragma unroll
    for (int j = 0; j < 16; ++j) v[j] = __builtin_bit_cast(cf, __builtin_amdgcn_raw_buffer_load_b64(rt, col, j * 16 * T_GSTR * 8, 16));
    asm volatile("" : "+v"(v[0]), "+v"(v[1]), "+v"(v[2]), "+v"(v[3]), "+v"(v[4]), "+v"(v[5]), "+v"(v[6]), "+v"(v[7]),
                 "+v"(v[8]), "+v"(v[9]), "+v"(v[10]), "+v"(v[11]), "+v"(v[12]), "+v"(v[13]), "+v"(v[14]), "+v"(v[15]));
    b2_wload(p, c, wv);
    r16<false, true>(v);
#pragma unroll
    for (int q = 0; q < 16; ++q) buf[q * C_RS + tid] = q ? cmul4(v[q], tw[q]) : v[q];
    if (c < 8) {
      asm volatile("" : "+v"(x) :: "memory");
      ((f32x4*)(buf + 16 * C_RS + 256))[tid] = x;
    }
  }
  __syncthreads();
  {
    cf* row = buf + ka * C_RS + e;
    cf v[16];
#pragma unroll
    for (int j = 0; j < 16; ++j) v[j] = LR(row + 16 * j);
    r16<false, true>(v);
    cf* ex = buf + 16 * C_RS;
    if (c < 2 && sp) {
#pragma unroll
      for (int i = 0; i < 16; ++i) ex[16 * i + ka] = v[i];
    }
    asm volatile("s_waitcnt vmcnt(0)" : "+v"(wv[0]), "+v"(wv[1]), "+v"(wv[2]), "+v"(wv[3]) :: "memory");
#pragma unroll
    for (int n = 0; n < 4; ++n) *(u32x4*)(wl + 1024 * n + 16 * (tid & 63)) = wv[n];
    WB();
#pragma unroll
    for (int i = 0; i < 16; ++i) {
      const uint16_t* q = (const uint16_t*)(wl + 128 * i + 2 * (tid & 63));
      wf[i] = mk(bf2f(q[0]), bf2f(q[1024]));
    }
    if (sp) {
#pragma unroll
      for (int i = 0; i < 16; ++i) wf[i] = *(const cf*)(pq + i);
    }
#pragma unroll
    for (int i = 0; i < 16; ++i) v[i] = cmul4(v[i], wf[i]);
    if (c < 2) {
      __syncthreads();
      if (sp) {
#pragma unroll
        for (int i = 0; i < 16; ++i) v[i] = cfma_cj4(v[i], ex[(256 - c - 16 * i - ka) & 255], ((const cf*)(pq + i))[1]);
      }
    }
    r16<true, true>(v);
#pragma unroll
    for (int j = 0; j < 16; ++j) row[16 * j] = j ? cmulc4(v[j], tw[j]) : v[j];
  }
  __syncthreads();
  {
    cf v[16];
#pragma unroll
    for (int q = 0; q < 16; ++q) v[q] = LR(buf + q * C_RS + tid);
    r16<true, true>(v);
#pragma unroll
    for (int j = 0; j < 16; ++j)
      __builtin_amdgcn_raw_buffer_store_b64(__builtin_bit_cast(u32x2, tag0(v[j])), rt, col, j * 16 * T_GSTR * 8, 16);
  }
}

DEV const uint16_t* pq_addr(const Params& p, int i) {
  const int z = threadIdx.x & 7, pos = 16 * i + ((threadIdx.x >> 3) & 15), e = pos >> 8;
  const int j = 128 * (pos & 15) + 8 * ((pos >> 4) & 15) + (e == 1 ? 4 : (e & 1) ? -(e >> 1) : (e >> 1));
  return ((z & 1) ? p.w_i : p.w_r) + (size_t)(((z & 2) ? -j : j) & 2047) * 513 + ((z & 4) ? 512 : 0);
}
DEV void pq_put(const Params& p, int i, uint32_t bits) {
  const int z = threadIdx.x & 7, pos = 16 * i + ((threadIdx.x >> 3) & 15), e = pos >> 8;
  const float f = bf2f(bits), x = (z & 3) == 3 ? -f : f;
  const float h = x + __shfl_xor(x, 2), o = __shfl_xor(h, 4);
  const float r = e < 2 ? ((z & 4) ? o - h : h + o) * 0.25f : ((e & 1) ? o : h) * 0.5f;
  if (!(z & 2)) st_c(p.cnt + SY_PQ + 4 * pos + (z & 1) + (z >> 1), __float_as_uint(r));
}

DEV cf cexp_rev(float a2, float th, float thl, float c) {
  const float mag = __builtin_amdgcn_exp2f(a2 * c);
  const float r = rintf(th * c);
  const float fr = fmaf(thl, c, fmaf(th, c, -r));
  return mk(mag * __builtin_amdgcn_cosf(fr), mag * __builtin_amdgcn_sinf(fr));
}
#define K2_SS 260
DEV void kg_put2(uint32_t* wr, int slot, cf a, cf b) {
  const uint32_t ha = cvt_pk(a.x, a.y), hb = cvt_pk(b.x, b.y);
  const cf ra = a - unpk(ha), rb = b - unpk(hb);
  const uint32_t la = cvt_pk(ra.x, ra.y), lb = cvt_pk(rb.x, rb.y);
  *(u32x2*)(wr + slot * K2_SS) = (u32x2){ha, hb};
  *(u32x2*)(wr + slot * K2_SS + 128) = (u32x2){la, lb};
}
DEV void kg_load2(const uint32_t* rd, u32x4 (*h)[8]) {
#pragma unroll
  for (int g = 0; g < 8; ++g) {
    h[g >> 2][g & 3] = *(const u32x4*)(rd + 16 * g);
    h[g >> 2][4 + (g & 3)] = *(const u32x4*)(rd + 128 + 16 * g);
  }
}
DEV f32x4 kg_mm(const u32x4* h, const u32x4* e) {
  f32x4 a0 = {0.f, 0.f, 0.f, 0.f}, a1 = a0;
#pragma unroll
  for (int g = 0; g < 4; ++g) {
    const bf16x8 hh = __builtin_bit_cast(bf16x8, h[g]), hl = __builtin_bit_cast(bf16x8, h[4 + g]);
    const bf16x8 eh = __builtin_bit_cast(bf16x8, e[g]), el = __builtin_bit_cast(bf16x8, e[4 + g]);
    a0 = __builtin_amdgcn_mfma_f32_16x16x32_bf16(hh, eh, a0, 0, 0, 0);
    a1 = __builtin_amdgcn_mfma_f32_16x16x32_bf16(hh, el, a1, 0, 0, 0);
    a1 = __builtin_amdgcn_mfma_f32_16x16x32_bf16(hl, eh, a1, 0, 0, 0);
    a1 = __builtin_amdgcn_mfma_f32_16x16x32_bf16(hl, el, a1, 0, 0, 0);
  }
  return a0 + a1;
}
#define PLO(y, x) __builtin_amdgcn_perm((y), (x), 0x05040100u)
#define PHI(y, x) __builtin_amdgcn_perm((y), (x), 0x07060302u)
DEV u32x4 kq_pack(f32x4 v) {
  u32x4 o;
#pragma unroll
  for (int i = 0; i < 4; i += 2) {
    const uint32_t h = cvt_pk(v[i], v[i + 1]);
    const cf r = mk(v[i], v[i + 1]) - unpk(h);
    const uint32_t l = cvt_pk(r.x, r.y);
    o[i] = PLO(l, h); o[i + 1] = PHI(l, h);
  }
  return o;
}
template <class F> DEV void role_kg(const Params& p, int d, uint32_t* sl, F&& hook) {
  const int lane = threadIdx.x & 63;
  const int q = lane >> 4, r = lane & 15;
  float a2[2], th[2], thl[2];
  cf c0[2];
  {
    uint32_t rw[6], ldt;
    const uint16_t* pa[6] = {p.a_re, p.a_im, p.b_re, p.b_im, p.c_re, p.c_im};
#pragma unroll
    for (int i = 0; i < 6; ++i) asm volatile("global_load_dword %0, %1, %2" : "=v"(rw[i]) : "v"(256 * d + 4 * lane), "s"(pa[i]) : "memory");
    asm volatile("global_load_ushort %0, %1, %2" : "=v"(ldt) : "v"(2 * d), "s"(p.log_dt) : "memory");
    asm volatile("s_waitcnt vmcnt(0)" : "+v"(rw[0]), "+v"(rw[1]), "+v"(rw[2]), "+v"(rw[3]), "+v"(rw[4]), "+v"(rw[5]), "+v"(ldt) :: "memory");
    hook(IC<0>{});
    const float dt = bfround((float)exp((double)bf2f(ldt)));
    const float I2PI_HI = 0.15915494309189535f;
    const float I2PI_LO = (float)(0.15915494309189533577 - (double)0.15915494309189535f);
#pragma unroll
    for (int h = 0; h < 2; ++h) {
#define PV(i) (h ? bfhi(rw[i]) : bflo(rw[i]))
      const float a = dt * PV(0), b = dt * PV(1);
      th[h] = b * I2PI_HI;
      thl[h] = fmaf(b, I2PI_HI, -th[h]) + b * I2PI_LO;
      a2[h] = a * 1.44269504088896341f;
      c0[h] = cmul(mk(PV(4), PV(5)), mk(dt * PV(2), dt * PV(3)));
#undef PV
    }
  }
  u32x4 e[2][8], hh[2][8];
  uint32_t* w2 = sl + 2 * lane;
  const uint32_t* r2 = sl + r * K2_SS + 4 * q;
  {
    cf E[2][16];
#pragma unroll
    for (int h = 0; h < 2; ++h) {
      cf p8 = cexp_rev(a2[h], th[h], thl[h], 8.f);
      p8.y = -p8.y;
      pow_tree15(p8, E[h]);
    }
    *(u32x2*)w2 = (u32x2){0x3f80u, 0x3f80u}; *(u32x2*)(w2 + 128) = (u32x2){0u, 0u};
#pragma unroll
    for (int j = 1; j < 16; ++j) kg_put2(w2, j, E[0][j], E[1][j]);
  }
  WB();
  kg_load2(r2, e);
  WB();
  cf P[2][8], M[2][8], p1024[2];
#pragma unroll
  for (int h = 0; h < 2; ++h) {
    const cf p1 = cexp_rev(a2[h], th[h], thl[h], 1.f);
    const cf p128 = cexp_rev(a2[h], th[h], thl[h], 128.f);
    p1024[h] = cexp_rev(a2[h], th[h], thl[h], 1024.f);
    pow_tree7(p1, P[h]);
    M[h][0] = c0[h];
    const cf p256 = cmul(p128, p128), p512 = cmul(p256, p256);
    M[h][1] = cmul(c0[h], p128);
    M[h][2] = cmul(c0[h], p256);
    M[h][3] = cmul(M[h][1], p256);
#pragma unroll
    for (int m = 0; m < 4; ++m) M[h][4 + m] = cmul(M[h][m], p512);
  }
#pragma unroll
  for (int m = 0; m < 2; ++m) {
    kg_put2(w2, 8 * m, M[0][m], M[1][m]);
#pragma unroll
    for (int i = 1; i < 8; ++i) kg_put2(w2, 8 * m + i, cmul(M[0][m], P[0][i]), cmul(M[1][m], P[1][i]));
  }
  WB();
  kg_load2(r2, hh);
  WB();
  hook(IC<1>{});
#pragma unroll
  for (int m = 0; m < 2; ++m) {
    kg_put2(w2, 4 * m, M[0][2 + m], M[1][2 + m]);
#pragma unroll
    for (int i = 1; i < 4; ++i) kg_put2(w2, 4 * m + i, cmul(M[0][2 + m], P[0][2 * i]), cmul(M[1][2 + m], P[1][2 * i]));
  }
#pragma unroll
  for (int m = 0; m < 4; ++m) {
    kg_put2(w2, 8 + 2 * m, M[0][4 + m], M[1][4 + m]);
    kg_put2(w2, 9 + 2 * m, cmul(M[0][4 + m], P[0][4]), cmul(M[1][4 + m], P[1][4]));
  }
  const f32x4 acc0 = kg_mm(hh[0], e[0]) + kg_mm(hh[1], e[1]);
  WB();
  kg_load2(r2, hh);
  WB();
  hook(IC<2>{});
  {
    const cf m8a = cmul(M[0][0], p1024[0]), m8b = cmul(M[1][0], p1024[1]);
    kg_put2(w2, 0, m8a, m8b);
#pragma unroll
    for (int m = 1; m < 8; ++m) kg_put2(w2, m, cmul(M[0][m], p1024[0]), cmul(M[1][m], p1024[1]));
    kg_put2(w2, 8, cmul(m8a, p1024[0]), cmul(m8b, p1024[1]));
  }
  const f32x4 acc1 = kg_mm(hh[0], e[0]) + kg_mm(hh[1], e[1]);
  WB();
  kg_load2(r2, hh);
  WB();
  const u32x4 s0 = kq_pack(acc0), s1 = kq_pack(acc1);
  *(u32x4*)(sl + 4 * (q & 1) + 128 * (q >> 1) + 8 * r) = s0;
  if (q < 2) {
    *(u32x2*)(sl + 256 + 32 * q + 2 * r) = (u32x2){s1[1], s1[3]};
    *(u32x2*)(sl + 320 + 32 * q + 2 * r) = (u32x2){s1[0], s1[2]};
  } else {
    *(u32x2*)(sl + 384 + 64 * (q - 2) + 2 * r) = (u32x2){s1[0], s1[1]};
    *(u32x2*)(sl + 416 + 64 * (q - 2) + 2 * r) = (u32x2){s1[2], s1[3]};
  }
  const u32x4 s2 = kq_pack(kg_mm(hh[0], e[0]) + kg_mm(hh[1], e[1]));
  if (q < 2) {
#pragma unroll
    for (int i = 0; i < 4; ++i) sl[512 + 16 * (4 * q + i) + r] = s2[i];
  } else if (lane == 32) sl[640] = s2[0];
  WB();
}

#define U_BS 288
#define M_UB 640
#define M_KD (M_UB + 16 * U_BS)
#define KD_LEN 592
#define M_TY (M_KD + 4 * KD_LEN)
#define T_LEN 448
#define M_TX (M_TY + 4 * T_LEN)
#define M_T3 (M_TX + 4 * T_LEN)
#define T3_LEN 176
#define M_CY (M_T3 + 8 * T3_LEN)
#define M_P (M_CY + 192)
#define M_R (M_P + 2112)
#define M_WB (M_P + 7424)
typedef __attribute__((ext_vector_type(4))) short s16x4;
typedef const volatile __attribute__((address_space(3))) u32x4* lp16;
typedef __attribute__((ext_vector_type(4), aligned(4))) uint32_t u32x4a;
typedef __attribute__((ext_vector_type(2), aligned(4))) uint32_t u32x2a;
typedef const volatile __attribute__((address_space(3))) u32x4a* lp4;
typedef const volatile __attribute__((address_space(3))) u32x2a* lp8;
typedef const volatile __attribute__((address_space(3))) u32x2* lp8a;
typedef const volatile __attribute__((address_space(3))) f32x4* lpf4;
typedef volatile __attribute__((address_space(3))) f32x4* lwf4;
typedef volatile __attribute__((address_space(3))) cf* lwf2;
typedef volatile __attribute__((address_space(3))) float* lwf1;
typedef __attribute__((address_space(3))) s16x4* lptr;
DEV int uaddr(int x) { return M_UB + 2 * x + 32 * (x >> 7); }
DEV uint32_t pin(uint32_t a) { asm volatile("" : "+v"(a)); return a; }
#define MF(d, a, b) asm volatile("v_mfma_f32_16x16x32_bf16 %0, %1, %2, %0" : "+v"(d) : "v"(a), "v"(b) : "memory")
#define MF0(d, a, b) asm volatile("v_mfma_f32_16x16x32_bf16 %0, %1, %2, 0" : "=&v"(d) : "v"(a), "v"(b) : "memory")
#define V2(op, d, a, b) asm volatile(op " %0, %1, %2" : "=v"(d) : "v"(a), "v"(b))
#define VSHR(d, a, b) asm volatile("v_add_f32_dpp %0, %1, %2 wave_shr:1 row_mask:0xf bank_mask:0xf bound_ctrl:0" : "=v"(d) : "v"(a), "v"(b))
DEV u32x4 am_bits(int lo, int len, int q) {
  const uint32_t bits = (((1u << len) - 1u) << lo) >> (8 * q);
  u32x4 o;
#pragma unroll
  for (int d = 0; d < 4; ++d) o[d] = ((bits >> (2 * d)) & 1u) | (((bits >> (2 * d + 1)) & 1u) << 16);
  return o;
}
DEV u32x4 am_ufrag(uint32_t a) {
  const s16x4 x = __builtin_amdgcn_ds_read_tr16_b64_v4i16((lptr)a), y = __builtin_amdgcn_ds_read_tr16_b64_v4i16((lptr)(a + 32));
  const u32x2 x2 = __builtin_bit_cast(u32x2, x), y2 = __builtin_bit_cast(u32x2, y);
  return (u32x4){x2[0], x2[1], y2[0], y2[1]};
}
DEV void st_b16(uint32_t a, uint32_t v, const int off, const int hs) {
  asm volatile("ds_write_b16 %0, %1 offset:%2\n\tds_write_b16_d16_hi %0, %1 offset:%3" :: "v"(a), "v"(v), "n"(off), "n"(off + hs) : "memory");
}
DEV void am_put(char* o, int st, const uint32_t* v) {
  *(u32x4*)o = (u32x4){PLO(v[7], v[8]), PLO(v[5], v[6]), PLO(v[3], v[4]), PLO(v[1], v[2])};
  *(u32x4*)(o + st) = (u32x4){PLO(v[6], v[7]), PLO(v[4], v[5]), PLO(v[2], v[3]), PLO(v[0], v[1])};
  *(u32x4*)(o + 2 * st) = (u32x4){PHI(v[7], v[8]), PHI(v[5], v[6]), PHI(v[3], v[4]), PHI(v[1], v[2])};
  *(u32x4*)(o + 3 * st) = (u32x4){PHI(v[6], v[7]), PHI(v[4], v[5]), PHI(v[2], v[3]), PHI(v[0], v[1])};
}
DEV uint32_t am_skip(float y0, float y1, uint32_t u2, float ds) {
  const uint32_t sk = cvt_pk(bflo(u2) * ds, bfhi(u2) * ds), yb = cvt_pk(y0, y1);
  return cvt_pk(bflo(yb) + bflo(sk), bfhi(yb) + bfhi(sk));
}
DEV void role_am(const Params& p, int g, char* lds) {
  const int tid = threadIdx.x, w = tid >> 6, lane = tid & 63;
  const int d = 4 * g + w;
  char* base = lds + w * M_WB;
  const uint32_t ta = (uint32_t)(size_t)(const __attribute__((address_space(3))) char*)base;
  const float ds = bf2f(p.dskip[d]);
  const int q = lane >> 4, m = lane & 15;
  uint32_t kf;
  {
    const uint32_t* kq = (const uint32_t*)base;
    const uint16_t* ug = p.u + (size_t)tid * 8192 + 4 * g;
    u32x2 ur[8];
    uint32_t pfu;
    role_kg(p, d, (uint32_t*)base, [&](auto hc) AINL {
      constexpr int h = decltype(hc)::v;
      if (h == 1) __builtin_amdgcn_s_setprio(3);
      if (h == 0) {
        const uint16_t* pf = p.u + (size_t)(64 * (g & 31) + (tid >> 2)) * 1024 + 128 * (g >> 5) + 32 * (tid & 3);
        asm volatile("global_load_dword %0, %1, off" : "=v"(pfu) : "v"(pf) : "memory");
      } else {
#pragma unroll
        for (int i = 4 * h - 4; i < 4 * h; ++i) ur[i] = *(const u32x2*)(ug + 1024 * i);
      }
    });
    const u32x4 ea = *(const u32x4*)(kq + 8 * lane), eb = *(const u32x4*)(kq + 8 * lane + 4);
    uint32_t em = kq[max(8 * lane - 1, 0)];
    const uint32_t e9 = kq[8 * lane + 8];
    const uint32_t x0 = kq[lane == 48 ? 319 : lane == 0 ? 255 : lane == 1 ? 511 : lane == 2 ? 512 : 8 * lane];
    const uint32_t x1 = kq[lane == 0 ? 320 : lane < 3 ? 512 : 8 * lane];
    const u32x2 e01 = *(const u32x2*)(kq + 512 + 2 * lane);
    const uint32_t e3[3] = {e01[0], e01[1], kq[514 + 2 * lane]};
    __syncthreads();
#pragma unroll
    for (int c = 0; c < 4; ++c) {
      u32x4 o;
#pragma unroll
      for (int i = 0; i < 4; ++i) o[i] = (c & 1) ? PHI(ur[2 * i + 1][c >> 1], ur[2 * i][c >> 1]) : PLO(ur[2 * i + 1][c >> 1], ur[2 * i][c >> 1]);
      *(u32x4*)(lds + c * M_WB + M_UB + 16 * tid + 32 * (tid >> 4)) = o;
    }
    asm volatile("" :: "v"(pfu));
    const u32x4 z4 = {0u, 0u, 0u, 0u};
    if (lane < 40) *(u32x4*)(base + 16 * lane) = z4;
#pragma unroll
    for (int i = 0; i < 8; ++i) *(u32x4*)(base + M_KD + 16 * lane + 1024 * i) = z4;
    if (lane == 0 || lane == 32 || lane == 40) em = 0u;
    const uint32_t v[9] = {em, lane == 40 ? 0u : ea[0], ea[1], ea[2], ea[3], eb[0], eb[1], eb[2], eb[3]};
    am_put(base + (lane < 32 ? M_KD + 16 * 35 : lane < 40 ? M_TY + 16 * 57 : M_TX + 16 * 65) - 16 * lane, lane < 32 ? KD_LEN : T_LEN, v);
    if (lane < 3 || lane >= 48) {
      const uint32_t zm = lane < 3 ? 0u : ~0u, y3 = ea[2] & zm, y5 = eb[0] & zm, y7 = eb[2] & zm;
      const uint32_t y[9] = {x0, x1, y3, y3, y5, y5, y7, y7, e9 & zm};
      am_put(base + (lane == 0 ? M_KD + 48 : lane == 1 ? M_TX + 16 : lane == 2 ? M_TY + 16 : M_TY + 16 * (65 - lane)), lane == 0 ? KD_LEN : T_LEN, y);
      if (lane == 0) { st_b16(ta, x1, M_KD + 60, 2 * KD_LEN); st_b16(ta, x1, M_KD + KD_LEN + 58, 2 * KD_LEN); }
    }
    const uint32_t tb = ta - 2 * lane;
    st_b16(tb, e3[1], M_T3 + 2 * 63, 2 * T3_LEN); st_b16(tb, e3[2], M_T3 + 4 * T3_LEN + 2 * 63, 2 * T3_LEN);
    if (lane < 63) { st_b16(tb, e3[1], M_T3 + T3_LEN + 2 * 62, 2 * T3_LEN); st_b16(tb, e3[2], M_T3 + 5 * T3_LEN + 2 * 62, 2 * T3_LEN); }
    kf = __builtin_amdgcn_readfirstlane(e3[0]);
  }
  u32x4 m0 = am_bits(m + 7, 7, q) * 0x3f80u, m1 = am_bits(m + 6, 9, q) * 0x3f80u;
  u32x4 fh = am_bits(m + 14, 2, q), fl = fh * (kf >> 16);
  fh *= kf & 0xffffu;
  const int tc = lane & 3, tr = (lane >> 2) & 3;
  const uint32_t ue = pin(ta + M_UB + 128 * tc + 32 * (tc >> 1) + 64 * (q & 1) + 8 * tr - 2 * U_BS);
  const uint32_t uo = pin(ta + M_UB + 128 * (tc + 1) + 32 * ((tc + 1) >> 1) + 64 * (q & 1) + 8 * tr - 2 * U_BS);
#define XUF(a) am_ufrag((((a) & 1) ? uo : ue) + U_BS * ((((a) + 4) >> 1)))
  const uint32_t typ = pin(ta + M_TY + ((m & 1) ? 0 : T_LEN - 2) + 2 * (15 - m + 8 * (q & 1)) + 2 * T_LEN * (q >> 1));
  const uint32_t txp = pin(ta + M_TX + ((m & 1) ? T_LEN - 2 : 0) + 2 * (14 - m + 8 * (q & 1)) + 2 * T_LEN * (q >> 1));
#define XTAP(pp, h) (*(lp4)((pp) + 32 * (12 - (h))))
  const uint32_t t3a = pin(ta + M_T3 + ((m & 1) ? 0 : T3_LEN - 2) + 2 * (15 - m + 4 * q)), t3b = pin(t3a + 4 * T3_LEN);
  const uint32_t kph = pin(ta + M_KD + ((m & 1) ? 0 : KD_LEN - 2) + 2 * (15 - m + 8 * q)), kpl = pin(kph + 2 * KD_LEN);
  const uint32_t u1 = pin(ta + U_BS * m + 16 * q), u2 = pin(u1 + (q >= 2 ? 32 : 0));
#define UFRAG(hh) (*(lp16)(((((hh) & 7) == 7) ? u2 : u1) + 32 * (hh) + 32 * ((hh) >> 3) + M_UB))
  const uint32_t d0 = pin(ta + uaddr(16 * m + 8 * q - 16)), d1 = pin(ta + uaddr(16 * m + 8 * q - 24));
  const uint32_t rw = pin(ta + M_R + 16 * (5 * m + q)), rr = pin(ta + M_R + 16 * (lane + (lane >> 2)));
  const uint32_t y0 = pin(ta + M_P + 16 * lane + 8 + 16 * (lane >> 5)), y1 = pin(ta + M_P + 16 * lane + 16 + 16 * ((lane + 1) >> 5));
  const uint32_t cw = pin(ta + (lane == 63 ? M_CY + 8 : M_T3 + 8 * lane));
  const uint32_t pr = pin(ta + (m >= 2 ? M_P + 528 * (m - 2) : 0) + 16 * q);
  const uint32_t cr = pin(ta + ((m >= 4 && !(m & 1) && q < 2) ? M_CY + 16 * (m - 4) + 16 * q : 0));
  asm volatile("s_waitcnt lgkmcnt(0)" : "+v"(m0), "+v"(m1), "+v"(fh), "+v"(fl) :: "memory");
  __syncthreads();
  __builtin_amdgcn_s_setprio(0);
  f32x4 acc[8], xa[7], ya[7], a3[4], bs[8], pv[8], cv;
  u32x4 dd[4], th[4], tl[4], tx[4][4], ty[4][4], uf[28], kh[9], kl[9], ua[2][12], ub[2][12];
  uint32_t sh[4][4], sl[4][4];
  float tt[8], ca_, cb_, cc_, o0, o1, o2, o3;
  {
    u32x4 de[4];
#pragma unroll
    for (int f = 0; f < 4; ++f) { dd[f] = *(lp16)(d0 + 2 * U_BS * f); de[f] = *(lp16)(d1 + 2 * U_BS * f); }
#pragma unroll
    for (int s = 0; s < 2; ++s) {
      kh[s] = *(lp4)(kph + 64 * (8 - s)); ua[0][s] = UFRAG(6 - 2 * s); ub[0][s] = UFRAG(5 - 2 * s); kl[s] = *(lp4)(kpl + 64 * (8 - s));
    }
#pragma unroll
    for (int f = 0; f < 4; ++f) { MF0(bs[2 * f], dd[f], m0); MF0(bs[2 * f + 1], de[f], m1); }
  }
  auto split = [&](int k) AINL {
    const int f = k / 24, r = k % 24;
    const int z = r < 4 ? 2 * r : r < 12 ? r - 4 : r < 20 ? r - 12 : 2 * (r - 20);
    const float x = bs[2 * f + (z >> 2)][z & 3];
    if (r < 4) V2("v_cvt_pk_bf16_f32", sh[f][r], x, bs[2 * f + (z >> 2)][(z & 3) + 1]);
    else if (r < 12 && !(z & 1)) asm volatile("v_lshlrev_b32 %0, 16, %1" : "=v"(tt[z]) : "v"(sh[f][z >> 1]));
    else if (r < 12) asm volatile("v_and_b32 %0, 0xffff0000, %1" : "=v"(tt[z]) : "v"(sh[f][z >> 1]));
    else if (r < 20) V2("v_sub_f32", tt[z], x, tt[z]);
    else V2("v_cvt_pk_bf16_f32", sl[f][r - 20], tt[z], tt[z + 1]);
  };
  auto side1 = [&](int s) AINL {
    const int k0 = (3 * (s - 2) + 1) / 2, k1 = (3 * (s - 1) + 1) / 2;
    if (s >= 2 && k0 < 96) { split(k0); if (k0 + 1 < k1) split(k0 + 1); }
  };
  auto comb = [&](int k) AINL {
    const int j = k / 10, st = k % 10;
    const f32x4 X = xa[j], Y = ya[j];
    if (st == 0) V2("v_add_f32", ca_, X[2], X[3]);
    if (st == 1) V2("v_add_f32", cc_, X[0], X[1]);
    if (st == 2) V2("v_add_f32", o1, Y[1], X[0]);
    if (st == 3) V2("v_add_f32", cb_, X[1], ca_);
    if (st == 4) V2("v_add_f32", o2, Y[2], cc_);
    if (st == 5) V2("v_add_f32", o3, Y[3], cc_);
    if (st == 6) VSHR(o0, cb_, Y[0]);
    if (st == 7) { VSHR(o1, ca_, o1); *(lwf2)(y0 + 1056 * j) = mk(o0, o1); }
    if (st == 8) VSHR(o2, X[3], o2);
    if (st == 9) {
      V2("v_add_f32", o3, o3, X[2]);
      *(lwf2)(y1 + 1056 * j) = mk(o2, o3);
      if (j < 6) { *(lwf2)(cw + 32 * j) = mk(cb_, ca_); *(lwf1)(cw + 32 * j + 8) = X[3]; }
    }
  };
  auto side2 = [&](int s) AINL {
    const int k0 = (5 * (s - 12) + 3) / 4, k1 = (5 * (s - 11) + 3) / 4;
    if (s >= 12 && k0 < 70) { comb(k0); if (k0 + 1 < k1 && k0 + 1 < 70) comb(k0 + 1); }
  };
  auto dstep = [&](auto hc, auto sc) AINL {
    constexpr int h = decltype(hc)::v, s = decltype(sc)::v, t0 = h ? 3 : 0, t1 = h ? 9 : 3;
    int n = 0, slot = 0;
#pragma unroll
    for (int z = t0; z < s; ++z) slot += 4 * ((z < t1 - 1 ? z : t1 - 1) - (z - 3 > t0 ? z - 3 : t0) + 1);
#pragma unroll
    for (int hl = 0; hl < 2; ++hl) {
#pragma unroll
      for (int c = 0; c < 4; ++c) {
        const int tau = s - c;
        if (tau < t0 || tau >= t1) continue;
#pragma unroll
        for (int e = 0; e < 2; ++e) {
          const int G = 7 - 2 * c - e;
          if (tau == 0 && hl == 0) MF0(acc[G], kh[tau], e ? ub[h][s] : ua[h][s]); else MF(acc[G], hl ? kl[tau] : kh[tau], e ? ub[h][s] : ua[h][s]);
          if (n == 0 && s + 2 < t1 + 3) ua[h][s + 2] = UFRAG(6 - 2 * (s + 2));
          if (n == 1 && s + 2 < t1 + 3) ub[h][s + 2] = UFRAG(5 - 2 * (s + 2));
          if (n == 2 && s + 2 < t1) kh[s + 2] = *(lp4)(kph + 64 * (6 - s));
          if (n == 3 && s + 2 < t1) kl[s + 2] = *(lp4)(kpl + 64 * (6 - s));
          ++n;
          if (h == 0) {
            if (slot >= 28 && slot < 44 && !(slot & 1)) {
              const int j = (slot - 28) >> 2, o = 32 * (3 - j) + ((slot & 2) ? 2 * T3_LEN : 0);
              const u32x2a a = *(lp8)(t3a + o), b = *(lp8)(t3b + o);
              if (slot & 2) tl[j] = (u32x4){a[0], a[1], b[0], b[1]}; else th[j] = (u32x4){a[0], a[1], b[0], b[1]};
            }
            side1(slot);
          } else {
            side2(44 + slot);
            if (slot >= 26 && slot < 34) pv[slot - 26] = *(lpf4)(pr + 64 * (slot - 26));
            if (slot == 34) cv = *(lpf4)cr;
            if (s == 7) {
              const int o = 8 * hl + 2 * c + e, Ga = 7 - ((o + 2) & 7);
              float z0 = acc[Ga][2 * hl], z1 = acc[Ga][2 * hl + 1];
              V2("v_add_f32", z0, z0, pv[Ga][2 * hl]); V2("v_add_f32", z1, z1, pv[Ga][2 * hl + 1]);
              if (Ga == 0) { V2("v_add_f32", z0, z0, cv[2 * hl]); V2("v_add_f32", z1, z1, cv[2 * hl + 1]); }
              acc[Ga][2 * hl] = z0; acc[Ga][2 * hl + 1] = z1;
            }
          }
          ++slot;
        }
      }
    }
  };
  sfor<0, 6>([&](auto sc) AINL { dstep(IC<0>{}, sc); });
  int slot = 48;
#pragma unroll
  for (int c = 0; c < 4; ++c) { MF0(a3[c], fh, dd[c]); side1(slot++); }
#pragma unroll
  for (int c = 0; c < 4; ++c) { MF(a3[c], fl, dd[c]); side1(slot++); }
#pragma unroll
  for (int f = 0; f < 4; ++f) {
#pragma unroll
    for (int k = 0; k < 3; ++k) {
#pragma unroll
      for (int c = f; c < 4; ++c) {
        const u32x4 sv = k == 2 ? (u32x4){sl[f][0], sl[f][1], sl[f][2], sl[f][3]} : (u32x4){sh[f][0], sh[f][1], sh[f][2], sh[f][3]};
        MF(a3[c], sv, k == 1 ? tl[c - f] : th[c - f]);
        if (f > 0 && f < 3 && k == 2 && c == 3) { *(lwf4)(rw + 1280 * (f - 1)) = a3[f - 1]; ya[2 + f] = *(lpf4)(rr + 1280 * (f - 1)); }
        if (f == 1 && c == 1) tx[3][k] = XTAP(txp, 4 * k + 3);
        if (f == 1 && c == 2) ty[3][k] = XTAP(typ, 4 * k + 3);
        if (f == 2 && c == 2 && k < 2) uf[k] = XUF(4 * k - 3);
        side1(slot++);
      }
    }
  }
  sfor<0, 4>([&](auto rc) AINL {
    constexpr int ph = decltype(rc)::v, r = 3 - ph, nh = r ? 3 : 4;
    int slot = 0;
#pragma unroll
    for (int b = 0; b < 7; ++b) {
#pragma unroll
      for (int fam = 0; fam < 2; ++fam) {
#pragma unroll
        for (int hh = 0; hh < nh; ++hh) {
          const int j = b + hh, i = 7 * ph + b;
          if (j > 6) continue;
          const bool first = ph == 0 && (b == 0 || hh == nh - 1);
          if (fam == 0) { if (first) MF0(xa[j], uf[i], tx[r][hh]); else MF(xa[j], uf[i], tx[r][hh]); }
          else { if (first && j < 3) MF0(ya[j], uf[i], ty[r][hh]); else MF(ya[j], uf[i], ty[r][hh]); }
          if (fam == 0 && hh == 0 && i + 2 < 28) uf[i + 2] = XUF(4 * ((i + 2) % 7) - 3 + (i + 2) / 7);
          if (ph == 0 && slot == 4) { *(lwf4)(rw + 1280 * 2) = a3[2]; ya[5] = *(lpf4)(rr + 1280 * 2); }
          if (ph == 0 && slot == 8) { *(lwf4)(rw + 1280 * 3) = a3[3]; ya[6] = *(lpf4)(rr + 1280 * 3); }
          if (ph < 3 && hh == 1 && b < (r == 1 ? 4 : 3)) { if (fam) ty[r - 1][b] = XTAP(typ, 4 * b + r - 1); else tx[r - 1][b] = XTAP(txp, 4 * b + r - 1); }
          if (ph == 3) {
            if (b >= 2 && b < 4 && hh == 1) { if (fam) kl[b + 1] = *(lp4)(kpl + 64 * (7 - b)); else kh[b + 1] = *(lp4)(kph + 64 * (7 - b)); }
            if (b >= 2 && b < 4 && hh == 2) { if (fam) ub[1][b + 1] = UFRAG(3 - 2 * b); else ua[1][b + 1] = UFRAG(4 - 2 * b); }
            side2(slot);
          }
          ++slot;
        }
      }
    }
  });
  sfor<3, 12>([&](auto sc) AINL { dstep(IC<1>{}, sc); });
#undef UFRAG
#undef XUF
#undef XTAP
  u32x2 us[8];
#pragma unroll
  for (int G = 0; G < 8; ++G) us[G] = *(lp8a)(base + M_UB + U_BS * m + 8 * q + 32 * G);
  asm volatile("s_nop 7\n\ts_nop 7");
#pragma unroll
  for (int i = 0; i < 8; ++i) asm volatile("" : "+v"(acc[i]));
#pragma unroll
  for (int G = 0; G < 8; ++G)
    *(u32x2*)(base + M_KD + 272 * m + 32 * G + 8 * q) = (u32x2){am_skip(acc[G][0], acc[G][1], us[G][0], ds), am_skip(acc[G][2], acc[G][3], us[G][1], ds)};
  __syncthreads();
  {
    u32x4 y[4];
#pragma unroll
    for (int c = 0; c < 4; ++c) y[c] = *(const u32x4*)(lds + c * M_WB + M_KD + 16 * tid + 16 * (tid >> 4));
    uint16_t* op = p.out + (size_t)(8 * tid) * 2048 + 4 * g;
#pragma unroll
    for (int z = 0; z < 8; ++z) {
      const u32x2 o = {(z & 1) ? PHI(y[1][z >> 1], y[0][z >> 1]) : PLO(y[1][z >> 1], y[0][z >> 1]),
                       (z & 1) ? PHI(y[3][z >> 1], y[2][z >> 1]) : PLO(y[3][z >> 1], y[2][z >> 1])};
      *(u32x2*)(op + z * 2048) = o;
      if (z < 6) asm volatile("s_waitcnt vmcnt(0)" ::: "memory");
    }
  }
}

#define K1 "_Z5k_all6Params"
#define KE "_Z6zz_endv"
#define code_line(A, B, slice) ({ uint64_t pc_; uint32_t off_, len_; \
    asm volatile("s_getpc_b64 %0\n1:\n\ts_mov_b32 %1, 1b-" A "\n\ts_mov_b32 %2, " B "-" A : "=s"(pc_), "=s"(off_), "=s"(len_)); \
    const uint32_t o_ = ((slice) * 256 + threadIdx.x) * 64; \
    (const char*)(pc_ - (int64_t)(int32_t)off_) + (o_ < len_ ? o_ : 0u); })
#define PF_LOAD(q) asm volatile("global_load_dword %0, %1, off" : "=v"(pfd) : "v"(q) : "memory")
#define L2_LDS (4 * M_WB)
#define B2_OFF (4 * B_SW * 8)
#define BX_OFF (B2_OFF + (16 * C_RS + 256) * 8 + 4096)
__global__ void __launch_bounds__(256, 2) k_all(Params p) {
  __shared__ __attribute__((aligned(16))) char lds[L2_LDS];
  const int b = blockIdx.x ^ 256;
  uint32_t pfd = 0;
  if (b < 40) PF_LOAD(code_line(K1, KE, b >> 3));
  if (b >= 40 && b < 48 && threadIdx.x < 52) PF_LOAD((const char*)TW - 256 + threadIdx.x * 64);
  if (b < 256) {
    role_am(p, (b & 7) * 32 + (b >> 3), lds);
  } else {
    __builtin_amdgcn_s_setprio(2);
    const int c = b - 256, tid = threadIdx.x, lane = tid & 63;
    const bool master = c == 0 && tid < 64;
    uint32_t* bx = (uint32_t*)(lds + BX_OFF);
    uint32_t* dn = p.cnt + SY_DN;
    uint32_t* rl = p.cnt + SY_RL;
    uint32_t n0, cv, c1, e[4], wsv, arr = 0;
    cf tw[16];
    BT bt;
    role_b1(p, c, (cf*)lds, bt, tw[1], [&](auto hc) AINL {
      constexpr int h = decltype(hc)::v;
      if (h == 0) {
        if (tid == 0) bx[0] = 0u;
        if (master) {
          LDC(cv, p.cnt + SY_B);
#pragma unroll
          for (int k = 0; k < 4; ++k) LDC(e[k], dn + 2 * (64 * k + lane));
        }
      }
      if (h == 1) {
        LDC(n0, rl + 4 * c);
        if (c < 128 && tid < 128) asm volatile("global_load_ushort %0, %1, off" : "=v"(wsv) : "v"(pq_addr(p, c)) : "memory");
        if (master) {
          asm volatile("s_waitcnt vmcnt(10)" : "+v"(cv), "+v"(e[0]), "+v"(e[1]), "+v"(e[2]), "+v"(e[3]) :: "memory");
          do ++cv; while (__ballot(e[0] == cv || e[1] == cv || e[2] == cv || e[3] == cv));
          if (tid == 0) { st_c(p.cnt + SY_B, cv); bx[2] = cv; }
        }
      }
      if (h == 2 && c < 128 && tid < 128) {
        asm volatile("s_waitcnt vmcnt(0)" : "+v"(wsv) :: "memory");
        pq_put(p, c, wsv);
      }
    });
    LDC(c1, p.cnt + SY_B);
    pow_tree15<true>(tw[1], tw);
    asm volatile("s_waitcnt vmcnt(0)" : "+v"(c1), "+v"(n0), "+v"(tw[1]), "+v"(tw[2]), "+v"(tw[3]), "+v"(tw[4]), "+v"(tw[5]), "+v"(tw[6]), "+v"(tw[7]), "+v"(tw[8]),
                 "+v"(tw[9]), "+v"(tw[10]), "+v"(tw[11]), "+v"(tw[12]), "+v"(tw[13]), "+v"(tw[14]), "+v"(tw[15]) :: "memory");
    cv = c ? c1 : bx[2];
    if (lane == 0) arr = atomicAdd(bx, 1u);
    arr = __builtin_amdgcn_readfirstlane(arr);
    ++n0;
    if (arr == 3 && lane == 0) st_c2(dn + 2 * c, cv, n0);
    if (master) {
      uint64_t a[4];
#pragma unroll 1
      for (int it = 0; it < SPIN_MAX; ++it) {
        bool ok = true;
#pragma unroll
        for (int k = 0; k < 4; ++k) { a[k] = ld_c2(dn + 2 * (64 * k + lane)); ok &= (uint32_t)a[k] == cv; }
        if (!__ballot(!ok)) break;
      }
#pragma unroll
      for (int k = 0; k < 4; ++k) st_c(rl + 4 * (64 * k + lane), (uint32_t)(a[k] >> 32));
    } else {
#pragma unroll 1
      for (int it = 0; it < SPIN_MAX; ++it) {
        const uint32_t r = ld_c(rl + 4 * c);
        const uint32_t c2 = ld_c(p.cnt + SY_B);
        asm volatile("" :: "v"(c2));
        if (r == n0) break;
        if (c2 != cv && arr == 3 && lane == 0) st_c2(dn + 2 * c, c2, n0);
        cv = c2;
        __builtin_amdgcn_s_sleep(1);
      }
    }
    asm volatile("" ::: "memory");
    role_b2(p, c, (cf*)(lds + B2_OFF), lds + (tid >> 6) * B_SW * 8, tw);
    role_b3(p, c, (cf*)lds, bt);
  }
  asm volatile("s_waitcnt vmcnt(0)" :: "v"(pfd));
}
__device__ __attribute__((used, noinline)) void zz_end() {}

extern "C" void kernel_launch(void* const* d_in, const int* in_sizes, int n_in,
                              void* d_out, int out_size, void* d_ws, size_t ws_size,
                              hipStream_t stream) {
  Params p;
  p.u = (const uint16_t*)d_in[0];
  p.a_re = (const uint16_t*)d_in[1];
  p.a_im = (const uint16_t*)d_in[2];
  p.b_re = (const uint16_t*)d_in[3];
  p.b_im = (const uint16_t*)d_in[4];
  p.c_re = (const uint16_t*)d_in[5];
  p.c_im = (const uint16_t*)d_in[6];
  p.log_dt = (const uint16_t*)d_in[7];
  p.dskip = (const uint16_t*)d_in[8];
  p.x = (const uint16_t*)d_in[9];
  p.w_r = (const uint16_t*)d_in[10];
  p.w_i = (const uint16_t*)d_in[11];
  p.out = (uint16_t*)d_out;
  char* ws = (char*)d_ws;
  p.T = (cf*)ws;
  p.cnt = (uint32_t*)(ws + T_BYTES);
  k_all<<<512, 256, 0, stream>>>(p);
}
```
